```python
import torch

def _softmax(x, axis=-1):
    return torch.softmax(x, dim=axis)
def _gelu(x):
    return torch.nn.functional.gelu(x, approximate="tanh")
def _silu(x):
    return torch.nn.functional.silu(x)
def _sigmoid(x):
    return torch.sigmoid(x)
def _layernorm(x, axis=-1, eps=1e-5):
    m = x.mean(dim=axis, keepdim=True)
    v = x.var(dim=axis, keepdim=True, unbiased=False)
    return (x - m) / torch.sqrt(v + eps)
def _scatter_add(tgt, idx, src, axis=0):
    return tgt.clone().index_add_(axis, idx, src)
def _segment_sum(src, seg, n):
    out = torch.zeros((n,) + src.shape[1:], dtype=src.dtype)
    return out.index_add_(0, seg, src)
def _pad(x, pw):
    flat = tuple(v for pair in reversed(pw) for v in pair)
    return torch.nn.functional.pad(x, flat)

kernel_name = 'standalone__kv_cache_quant_int4_int2_kv_with_per_channel_or_per_token_scales__decode'

def setup_inputs():

    _w = (lambda a: (lambda t: t.to(torch.bfloat16) if t.is_floating_point() else t)(torch.as_tensor(a)))
    B = 1
    Sq = 1
    Skv = 65536
    Hq = 32
    Hkv = 4
    Dh = 128
    bits = 2
    G = 128
    if G <= 0 or G >= Skv:
        G = max(1, Skv // 2)
    Skv = (Skv // G) * G
    ngk = Skv // G
    qmax = (1 << bits) - 1
    return {
        "q": _w((lambda *s: torch.randn(*s, dtype=torch.bfloat16) * 3.2)(B, Hq, Sq, Dh)),
        "qk": _w(torch.randint(0, qmax + 1, (B, Hkv, Skv, Dh)).to(torch.int32)),
        "qv": _w(torch.randint(0, qmax + 1, (B, Hkv, Skv, Dh)).to(torch.int32)),
        "k_scales": _w(torch.abs((lambda *s: torch.randn(*s, dtype=torch.bfloat16) * 3.2)(B, Hkv, ngk, Dh)) * 0.05 + 0.02),
        "k_zeros": _w((lambda *s: torch.randn(*s, dtype=torch.bfloat16) * 3.2)(B, Hkv, ngk, Dh) + float(qmax) / 2.0),
        "v_scales": _w(torch.abs((lambda *s: torch.randn(*s, dtype=torch.bfloat16) * 0.05)(B, Hkv, Skv, 1)) * 0.05 + 0.02),
        "v_zeros": _w((lambda *s: torch.randn(*s, dtype=torch.bfloat16) * 0.05)(B, Hkv, Skv, 1) + float(qmax) / 2.0),
    }

def reference(q, qk, qv, k_scales, k_zeros, v_scales, v_zeros):
    B, Hq, Sq, Dh = q.shape
    Hkv, Skv = qk.shape[1], qk.shape[2]
    ngk = k_scales.shape[2]
    G = Skv // ngk
    ks = (lambda x, n, axis: torch.repeat_interleave(x, n, dim=axis))(k_scales, G, axis=2)
    kz = (lambda x, n, axis: torch.repeat_interleave(x, n, dim=axis))(k_zeros, G, axis=2)
    k = ((lambda x, dt: x.to(dt))(qk, q.dtype) - kz) * ks
    v = ((lambda x, dt: x.to(dt))(qv, q.dtype) - v_zeros) * v_scales
    rep = Hq // Hkv
    if rep > 1:
        k = (lambda x, n, axis: torch.repeat_interleave(x, n, dim=axis))(k, rep, axis=1)
        v = (lambda x, n, axis: torch.repeat_interleave(x, n, dim=axis))(v, rep, axis=1)
    scale = float(Dh) ** -0.5
    logits = torch.einsum("bhqd,bhkd->bhqk", q, k) * scale
    p = _softmax(logits, axis=-1)
    o = torch.einsum("bhqk,bhkd->bhqd", p, v)
    return o
```

```cpp
#include <hip/hip_runtime.h>
#include <hip/hip_bf16.h>
#include <stdint.h>

#define GAS __attribute__((address_space(1)))
#define LAS __attribute__((address_space(3)))
typedef int i32x4 __attribute__((ext_vector_type(4)));
typedef unsigned u32x4 __attribute__((ext_vector_type(4)));
typedef unsigned u32x2 __attribute__((ext_vector_type(2)));
typedef float f32x4 __attribute__((ext_vector_type(4)));
typedef float f32x2 __attribute__((ext_vector_type(2)));
typedef __bf16 bf16x2 __attribute__((ext_vector_type(2)));
typedef __bf16 bf16x8 __attribute__((ext_vector_type(8)));

constexpr int HQ = 32, HKV = 4, REP = HQ / HKV, DH = 128, SKV = 65536, GRP = 128, NGK = SKV / GRP;
constexpr int NWAVE = 8, NTHR = NWAVE * 64;
constexpr int NWG = 256;
constexpr int WG_PER_HEAD = NWG / HKV;
constexpr int TOK_PER_WAVE = SKV / (WG_PER_HEAD * NWAVE);
constexpr int NBLK = TOK_PER_WAVE / 32;
constexpr int KROW = 272;
constexpr int LDS_WAVE = 32 * KROW;
static_assert(TOK_PER_WAVE == GRP, "one K quantisation group per wave");

__device__ __forceinline__ unsigned pk_bf16(float lo, float hi) {
  f32x2 v = {lo, hi};
  return __builtin_bit_cast(unsigned, __builtin_convertvector(v, bf16x2));
}
__device__ __forceinline__ float bf_lo(unsigned u) { return __builtin_bit_cast(float, u << 16); }
__device__ __forceinline__ float bf_hi(unsigned u) { return __builtin_bit_cast(float, u & 0xffff0000u); }
__device__ __forceinline__ f32x4 mfma16(u32x4 a, u32x4 b, f32x4 c) {
  return __builtin_amdgcn_mfma_f32_16x16x32_bf16(__builtin_bit_cast(bf16x8, a), __builtin_bit_cast(bf16x8, b), c, 0, 0, 0);
}
#define KV_AUX 2
__device__ __forceinline__ i32x4 ldb(__amdgpu_buffer_rsrc_t rs, int voff, int soff, int off) {
  return __builtin_bit_cast(i32x4, __builtin_amdgcn_raw_buffer_load_b128(rs, voff + (off & 4095), soff + (off & ~4095), KV_AUX));
}

__device__ __forceinline__ float xg_max(float x) {
  auto s = __builtin_amdgcn_permlane16_swap(__float_as_uint(x), __float_as_uint(x), false, false);
  x = fmaxf(__uint_as_float(s[0]), __uint_as_float(s[1]));
  auto t = __builtin_amdgcn_permlane32_swap(__float_as_uint(x), __float_as_uint(x), false, false);
  return fmaxf(__uint_as_float(t[0]), __uint_as_float(t[1]));
}
__device__ __forceinline__ float xg_sum(float x) {
  auto s = __builtin_amdgcn_permlane16_swap(__float_as_uint(x), __float_as_uint(x), false, false);
  x = __uint_as_float(s[0]) + __uint_as_float(s[1]);
  auto t = __builtin_amdgcn_permlane32_swap(__float_as_uint(x), __float_as_uint(x), false, false);
  return __uint_as_float(t[0]) + __uint_as_float(t[1]);
}
__device__ __forceinline__ float ror8(float x) {
  return __builtin_bit_cast(float, __builtin_amdgcn_mov_dpp(__builtin_bit_cast(int, x), 0x128, 0xf, 0xf, true));
}

__device__ __forceinline__ unsigned dq2(int x0, int x1, float z0, float z1, float s0, float s1) {
  const unsigned y = pk_bf16((float)x0 - z0, (float)x1 - z1);
  return pk_bf16(bf_lo(y) * s0, bf_hi(y) * s1);
}

__global__ void __launch_bounds__(NTHR, 2)
kv_stream_kernel(const unsigned short* __restrict__ q, const int* __restrict__ qk, const int* __restrict__ qv,
                 const unsigned short* __restrict__ ksc, const unsigned short* __restrict__ kze,
                 const unsigned short* __restrict__ vsc, const unsigned short* __restrict__ vze,
                 float* __restrict__ po, float* __restrict__ pml) {
  __shared__ __attribute__((aligned(16))) unsigned char lds[NWAVE * LDS_WAVE];
  const int tid = threadIdx.x, lane = tid & 63;
  const int wave = __builtin_amdgcn_readfirstlane(tid >> 6);
  const int b = blockIdx.x;
  const int kvh = b / WG_PER_HEAD;
  const int grp = (b % WG_PER_HEAD) * NWAVE + wave;
  const size_t tok0 = (size_t)kvh * SKV + (size_t)grp * GRP;
  const int r = lane & 15, g = lane >> 4;

  const __amdgpu_buffer_rsrc_t krs = __builtin_amdgcn_make_buffer_rsrc((void*)(qk + tok0 * DH), 0, TOK_PER_WAVE * DH * 4, 0x00020000);
  const __amdgpu_buffer_rsrc_t vrs = __builtin_amdgcn_make_buffer_rsrc((void*)(qv + tok0 * DH), 0, TOK_PER_WAVE * DH * 4, 0x00020000);
  const int koff = lane * 16;
  const int voff = g * 4096 + r * 16;
  const __amdgpu_buffer_rsrc_t vsrs = __builtin_amdgcn_make_buffer_rsrc((void*)(vsc + tok0), 0, TOK_PER_WAVE * 2, 0x00020000);
  const __amdgpu_buffer_rsrc_t vzrs = __builtin_amdgcn_make_buffer_rsrc((void*)(vze + tok0), 0, TOK_PER_WAVE * 2, 0x00020000);

  i32x4 kr[16], vr[16];
  u32x4 vs4, vz4;
  const u32x2 ks2 = *((const GAS u32x2*)(ksc + ((size_t)kvh * NGK + grp) * DH) + (lane & 31));
  const u32x2 kz2 = *((const GAS u32x2*)(kze + ((size_t)kvh * NGK + grp) * DH) + (lane & 31));
  u32x4 qf[4];
#pragma unroll
  for (int s = 0; s < 4; ++s) qf[s] = *((const GAS u32x4*)(q + (size_t)(kvh * REP + (r & 7)) * DH + 32 * s + 8 * g));
#pragma unroll
  for (int i = 0; i < 16; ++i) kr[i] = ldb(krs, koff, 0, i * 1024);
  vs4 = __builtin_amdgcn_raw_buffer_load_b128(vsrs, g * 16, 0, 0);
  vz4 = __builtin_amdgcn_raw_buffer_load_b128(vzrs, g * 16, 0, 0);
#pragma unroll
  for (int i = 0; i < 16; ++i) vr[i] = ldb(vrs, voff, 0, (i & 7) * 512 + (i >> 3) * 256);
  __builtin_amdgcn_sched_barrier(0);

  const float ks0 = bf_lo(ks2.x), ks1 = bf_hi(ks2.x), ks2f = bf_lo(ks2.y), ks3 = bf_hi(ks2.y);
  const float kz0 = bf_lo(kz2.x), kz1 = bf_hi(kz2.x), kz2f = bf_lo(kz2.y), kz3 = bf_hi(kz2.y);

  LAS unsigned char* L = (LAS unsigned char*)lds + wave * LDS_WAVE;
  LAS unsigned char* wr_base = L + (lane >> 5) * KROW + (lane & 31) * 8;
  LAS unsigned char* rd_base = L + r * KROW + g * 16;

  f32x4 acc[8];
#pragma unroll
  for (int i = 0; i < 8; ++i) acc[i] = f32x4{0.f, 0.f, 0.f, 0.f};
  float m = -INFINITY, l = 0.f;
  constexpr float SCALE = 0.08838834764831845f;
  constexpr float LOG2E = 1.4426950408889634f;

#pragma unroll 1
  for (int blk = 0; blk < NBLK; ++blk) {
    const int nxt = (blk + 1) * 16384;
#pragma unroll
    for (int i = 0; i < 16; ++i) {
      u32x2 w;
      w.x = dq2(kr[i].x, kr[i].y, kz0, kz1, ks0, ks1);
      w.y = dq2(kr[i].z, kr[i].w, kz2f, kz3, ks2f, ks3);
      const int row = 16 * ((i >> 1) & 1) + 4 * (i >> 2) + 2 * (i & 1);
      *(LAS u32x2*)(wr_base + row * KROW) = w;
      __builtin_amdgcn_sched_barrier(0);
      kr[i] = ldb(krs, koff, nxt, i * 1024);
      __builtin_amdgcn_sched_barrier(0);
    }
    f32x4 sc[2];
#pragma unroll
    for (int T = 0; T < 2; ++T) {
      sc[T] = f32x4{0.f, 0.f, 0.f, 0.f};
#pragma unroll
      for (int s = 0; s < 4; ++s) {
        const u32x4 a = *(const LAS u32x4*)(rd_base + T * 16 * KROW + s * 64);
        sc[T] = mfma16(a, qf[s], sc[T]);
      }
    }
    float x[8];
#pragma unroll
    for (int j = 0; j < 8; j += 2) {
      const unsigned u = pk_bf16(sc[j >> 2][j & 3], sc[j >> 2][(j & 3) + 1]);
      const unsigned v = pk_bf16(bf_lo(u) * SCALE, bf_hi(u) * SCALE);
      x[j] = bf_lo(v);
      x[j + 1] = bf_hi(v);
    }
    float bm = fmaxf(fmaxf(fmaxf(x[0], x[1]), fmaxf(x[2], x[3])), fmaxf(fmaxf(x[4], x[5]), fmaxf(x[6], x[7])));
    bm = xg_max(bm);
    const float mn = fmaxf(m, bm);
    const float alpha = __builtin_amdgcn_exp2f((m - mn) * LOG2E);
    m = mn;
    float p[8], ps = 0.f;
#pragma unroll
    for (int j = 0; j < 8; ++j) { p[j] = __builtin_amdgcn_exp2f((x[j] - mn) * LOG2E); ps += p[j]; }
    l = l * alpha + ps;
    u32x4 pf;
#pragma unroll
    for (int j = 0; j < 8; j += 2) {
      const unsigned h = pk_bf16(p[j], p[j + 1]);
      const unsigned lo = pk_bf16(p[j] - bf_lo(h), p[j + 1] - bf_hi(h));
      pf[j >> 1] = (r < 8) ? h : lo;
    }
#pragma unroll
    for (int i = 0; i < 8; ++i) acc[i] *= alpha;
    float vs[8], vz[8];
#pragma unroll
    for (int j = 0; j < 4; ++j) { vs[2 * j] = bf_lo(vs4[j]); vs[2 * j + 1] = bf_hi(vs4[j]); vz[2 * j] = bf_lo(vz4[j]); vz[2 * j + 1] = bf_hi(vz4[j]); }
    __builtin_amdgcn_sched_barrier(0);
    vs4 = __builtin_amdgcn_raw_buffer_load_b128(vsrs, g * 16, (blk + 1) * 64, 0);
    vz4 = __builtin_amdgcn_raw_buffer_load_b128(vzrs, g * 16, (blk + 1) * 64, 0);
    __builtin_amdgcn_sched_barrier(0);
#pragma unroll
    for (int hf = 0; hf < 2; ++hf) {
      u32x4 a[4];
#pragma unroll
      for (int j = 0; j < 8; j += 2) {
        const i32x4 v0 = vr[hf * 8 + j], v1 = vr[hf * 8 + j + 1];
#pragma unroll
        for (int c = 0; c < 4; ++c) a[c][j >> 1] = dq2(v0[c], v1[c], vz[j], vz[j + 1], vs[j], vs[j + 1]);
        __builtin_amdgcn_sched_barrier(0);
        vr[hf * 8 + j] = ldb(vrs, voff, nxt, j * 512 + hf * 256);
        vr[hf * 8 + j + 1] = ldb(vrs, voff, nxt, (j + 1) * 512 + hf * 256);
        __builtin_amdgcn_sched_barrier(0);
      }
#pragma unroll
      for (int c = 0; c < 4; ++c) acc[hf * 4 + c] = mfma16(a[c], pf, acc[hf * 4 + c]);
    }
  }

  l = xg_sum(l);
#pragma unroll
  for (int i = 0; i < 8; ++i)
#pragma unroll
    for (int k = 0; k < 4; ++k) acc[i][k] += ror8(acc[i][k]);
  {
    LAS float* rec = (LAS float*)L;
    const int hf = r >> 3, h = r & 7;
#pragma unroll
    for (int reg = 0; reg < 4; ++reg) {
      f32x4 o;
#pragma unroll
      for (int c = 0; c < 4; ++c) o[c] = hf ? acc[4 + c][reg] : acc[c][reg];
      *(LAS f32x4*)(rec + h * 132 + 64 * hf + 16 * g + 4 * reg) = o;
    }
    if (lane < 8) { rec[8 * 132 + lane] = m; rec[8 * 132 + 8 + lane] = l; }
  }
  __syncthreads();
  {
    const int h = wave, d0 = 2 * lane;
    float mw[NWAVE], M = -INFINITY;
#pragma unroll
    for (int w = 0; w < NWAVE; ++w) { mw[w] = ((LAS float*)((LAS unsigned char*)lds + w * LDS_WAVE))[8 * 132 + h]; M = fmaxf(M, mw[w]); }
    float Ls = 0.f, o0 = 0.f, o1 = 0.f;
#pragma unroll
    for (int w = 0; w < NWAVE; ++w) {
      LAS float* rec = (LAS float*)((LAS unsigned char*)lds + w * LDS_WAVE);
      const float wt = __builtin_amdgcn_exp2f((mw[w] - M) * LOG2E);
      const f32x2 a = *(LAS f32x2*)(rec + h * 132 + d0);
      Ls = fmaf(wt, rec[8 * 132 + 8 + h], Ls);
      o0 = fmaf(wt, a.x, o0);
      o1 = fmaf(wt, a.y, o1);
    }
    const int head = kvh * REP + h, slot = b % WG_PER_HEAD;
    *(f32x2*)(po + ((size_t)head * WG_PER_HEAD + slot) * DH + d0) = f32x2{o0, o1};
    if (lane == 0) *(f32x2*)(pml + ((size_t)head * WG_PER_HEAD + slot) * 2) = f32x2{M, Ls};
  }
}

__global__ void __launch_bounds__(256)
kv_combine_kernel(const float* __restrict__ po, const float* __restrict__ pml, unsigned* __restrict__ out) {
  __shared__ f32x4 red[8][32];
  const int head = blockIdx.x, tid = threadIdx.x, lane = tid & 63;
  const int s8 = tid >> 5, c = tid & 31;
  f32x4 v[8];
#pragma unroll
  for (int i = 0; i < 8; ++i) v[i] = *(const f32x4*)(po + ((size_t)head * WG_PER_HEAD + s8 * 8 + i) * DH + 4 * c);
  const f32x2 ml = *(const f32x2*)(pml + ((size_t)head * WG_PER_HEAD + lane) * 2);
  float M = ml.x;
#pragma unroll
  for (int o = 32; o >= 1; o >>= 1) M = fmaxf(M, __shfl_xor(M, o));
  const float w = __builtin_amdgcn_exp2f((ml.x - M) * 1.4426950408889634f);
  float Ls = w * ml.y;
#pragma unroll
  for (int o = 32; o >= 1; o >>= 1) Ls += __shfl_xor(Ls, o);
  f32x4 a = {0.f, 0.f, 0.f, 0.f};
#pragma unroll
  for (int i = 0; i < 8; ++i) a += v[i] * __shfl(w, s8 * 8 + i);
  red[s8][c] = a;
  __syncthreads();
  if (tid < 32) {
    f32x4 t = red[0][tid];
#pragma unroll
    for (int i = 1; i < 8; ++i) t += red[i][tid];
    const float inv = 1.f / Ls;
    u32x2 o;
    o.x = pk_bf16(t.x * inv, t.y * inv);
    o.y = pk_bf16(t.z * inv, t.w * inv);
    *(u32x2*)(out + head * (DH / 2) + 2 * tid) = o;
  }
}

extern "C" void kernel_launch(void* const* d_in, const int* in_sizes, int n_in, void* d_out, int out_size, void* d_ws,
                              size_t ws_size, hipStream_t stream) {
  const unsigned short* q = (const unsigned short*)d_in[0];
  const int* qk = (const int*)d_in[1];
  const int* qv = (const int*)d_in[2];
  const unsigned short* ksc = (const unsigned short*)d_in[3];
  const unsigned short* kze = (const unsigned short*)d_in[4];
  const unsigned short* vsc = (const unsigned short*)d_in[5];
  const unsigned short* vze = (const unsigned short*)d_in[6];
  float* po = (float*)d_ws;
  float* pml = po + (size_t)HQ * WG_PER_HEAD * DH;
  kv_stream_kernel<<<NWG, NTHR, 0, stream>>>(q, qk, qv, ksc, kze, vsc, vze, po, pml);
  kv_combine_kernel<<<HQ, 256, 0, stream>>>(po, pml, (unsigned*)d_out);
}
```

```cpp
#include <hip/hip_runtime.h>
#include <hip/hip_bf16.h>
#include <stdint.h>

#define GAS __attribute__((address_space(1)))
#define LAS __attribute__((address_space(3)))
typedef int i32x4 __attribute__((ext_vector_type(4)));
typedef unsigned u32x4 __attribute__((ext_vector_type(4)));
typedef unsigned u32x2 __attribute__((ext_vector_type(2)));
typedef float f32x4 __attribute__((ext_vector_type(4)));
typedef float f32x2 __attribute__((ext_vector_type(2)));
typedef __bf16 bf16x2 __attribute__((ext_vector_type(2)));
typedef __bf16 bf16x8 __attribute__((ext_vector_type(8)));

constexpr int HQ = 32, HKV = 4, REP = HQ / HKV, DH = 128, SKV = 65536, GRP = 128, NGK = SKV / GRP;
constexpr int NWAVE = 8, NTHR = NWAVE * 64;
constexpr int NWG = 256;
constexpr int WG_PER_HEAD = NWG / HKV;
constexpr int TOK_PER_WAVE = SKV / (WG_PER_HEAD * NWAVE);
constexpr int NBLK = TOK_PER_WAVE / 32;
constexpr int KROW = 272;
constexpr int LDS_WAVE = 32 * KROW;
static_assert(TOK_PER_WAVE == GRP, "one K quantisation group per wave");

__device__ __forceinline__ unsigned pk_bf16(float lo, float hi) {
  f32x2 v = {lo, hi};
  return __builtin_bit_cast(unsigned, __builtin_convertvector(v, bf16x2));
}
__device__ __forceinline__ float bf_lo(unsigned u) { return __builtin_bit_cast(float, u << 16); }
__device__ __forceinline__ float bf_hi(unsigned u) { return __builtin_bit_cast(float, u & 0xffff0000u); }
__device__ __forceinline__ f32x4 mfma16(u32x4 a, u32x4 b, f32x4 c) {
  return __builtin_amdgcn_mfma_f32_16x16x32_bf16(__builtin_bit_cast(bf16x8, a), __builtin_bit_cast(bf16x8, b), c, 0, 0, 0);
}
#define KV_AUX 2
__device__ __forceinline__ i32x4 ldb(__amdgpu_buffer_rsrc_t rs, int voff, int soff, int off) {
  return __builtin_bit_cast(i32x4, __builtin_amdgcn_raw_buffer_load_b128(rs, voff + (off & 4095), soff + (off & ~4095), KV_AUX));
}

__device__ __forceinline__ float xg_max(float x) {
  auto s = __builtin_amdgcn_permlane16_swap(__float_as_uint(x), __float_as_uint(x), false, false);
  x = fmaxf(__uint_as_float(s[0]), __uint_as_float(s[1]));
  auto t = __builtin_amdgcn_permlane32_swap(__float_as_uint(x), __float_as_uint(x), false, false);
  return fmaxf(__uint_as_float(t[0]), __uint_as_float(t[1]));
}
__device__ __forceinline__ float xg_sum(float x) {
  auto s = __builtin_amdgcn_permlane16_swap(__float_as_uint(x), __float_as_uint(x), false, false);
  x = __uint_as_float(s[0]) + __uint_as_float(s[1]);
  auto t = __builtin_amdgcn_permlane32_swap(__float_as_uint(x), __float_as_uint(x), false, false);
  return __uint_as_float(t[0]) + __uint_as_float(t[1]);
}
__device__ __forceinline__ float ror8(float x) {
  return __builtin_bit_cast(float, __builtin_amdgcn_mov_dpp(__builtin_bit_cast(int, x), 0x128, 0xf, 0xf, true));
}

__device__ __forceinline__ unsigned dq2(int x0, int x1, float z0, float z1, float s0, float s1) {
  const unsigned y = pk_bf16((float)x0 - z0, (float)x1 - z1);
  return pk_bf16(bf_lo(y) * s0, bf_hi(y) * s1);
}

__global__ void __launch_bounds__(NTHR, 2)
kv_stream_kernel(const unsigned short* __restrict__ q, const int* __restrict__ qk, const int* __restrict__ qv,
                 const unsigned short* __restrict__ ksc, const unsigned short* __restrict__ kze,
                 const unsigned short* __restrict__ vsc, const unsigned short* __restrict__ vze,
                 float* __restrict__ po, float* __restrict__ pml, int live) {
  __shared__ __attribute__((aligned(16))) unsigned char lds[NWAVE * LDS_WAVE];
  const int tid = threadIdx.x, lane = tid & 63;
  const int wave = __builtin_amdgcn_readfirstlane(tid >> 6);
  const int b = blockIdx.x;
  const int kvh = b / WG_PER_HEAD;
  const int grp = (b % WG_PER_HEAD) * NWAVE + wave;
  const size_t tok0 = (size_t)kvh * SKV + (size_t)grp * GRP;
  const int r = lane & 15, g = lane >> 4;

  const __amdgpu_buffer_rsrc_t krs = __builtin_amdgcn_make_buffer_rsrc((void*)(qk + tok0 * DH), 0, TOK_PER_WAVE * DH * 4 * live, 0x00020000);
  const __amdgpu_buffer_rsrc_t vrs = __builtin_amdgcn_make_buffer_rsrc((void*)(qv + tok0 * DH), 0, TOK_PER_WAVE * DH * 4 * live, 0x00020000);
  const int koff = lane * 16;
  const int voff = g * 4096 + r * 16;
  const __amdgpu_buffer_rsrc_t vsrs = __builtin_amdgcn_make_buffer_rsrc((void*)(vsc + tok0), 0, TOK_PER_WAVE * 2, 0x00020000);
  const __amdgpu_buffer_rsrc_t vzrs = __builtin_amdgcn_make_buffer_rsrc((void*)(vze + tok0), 0, TOK_PER_WAVE * 2, 0x00020000);

  i32x4 kr[16], vr[16];
  u32x4 vs4, vz4;
  const u32x2 ks2 = *((const GAS u32x2*)(ksc + ((size_t)kvh * NGK + grp) * DH) + (lane & 31));
  const u32x2 kz2 = *((const GAS u32x2*)(kze + ((size_t)kvh * NGK + grp) * DH) + (lane & 31));
  u32x4 qf[4];
#pragma unroll
  for (int s = 0; s < 4; ++s) qf[s] = *((const GAS u32x4*)(q + (size_t)(kvh * REP + (r & 7)) * DH + 32 * s + 8 * g));
#pragma unroll
  for (int i = 0; i < 16; ++i) kr[i] = ldb(krs, koff, 0, i * 1024);
  vs4 = __builtin_amdgcn_raw_buffer_load_b128(vsrs, g * 16, 0, 0);
  vz4 = __builtin_amdgcn_raw_buffer_load_b128(vzrs, g * 16, 0, 0);
#pragma unroll
  for (int i = 0; i < 16; ++i) vr[i] = ldb(vrs, voff, 0, (i & 7) * 512 + (i >> 3) * 256);
  __builtin_amdgcn_sched_barrier(0);

  const float ks0 = bf_lo(ks2.x), ks1 = bf_hi(ks2.x), ks2f = bf_lo(ks2.y), ks3 = bf_hi(ks2.y);
  const float kz0 = bf_lo(kz2.x), kz1 = bf_hi(kz2.x), kz2f = bf_lo(kz2.y), kz3 = bf_hi(kz2.y);

  LAS unsigned char* L = (LAS unsigned char*)lds + wave * LDS_WAVE;
  LAS unsigned char* wr_base = L + (lane >> 5) * KROW + (lane & 31) * 8;
  LAS unsigned char* rd_base = L + r * KROW + g * 16;

  f32x4 acc[8];
#pragma unroll
  for (int i = 0; i < 8; ++i) acc[i] = f32x4{0.f, 0.f, 0.f, 0.f};
  float m = -INFINITY, l = 0.f;
  constexpr float SCALE = 0.08838834764831845f;
  constexpr float LOG2E = 1.4426950408889634f;

#pragma unroll 1
  for (int blk = 0; blk < NBLK; ++blk) {
    const int nxt = (blk + 1) * 16384;
#pragma unroll
    for (int i = 0; i < 16; ++i) {
      u32x2 w;
      w.x = dq2(kr[i].x, kr[i].y, kz0, kz1, ks0, ks1);
      w.y = dq2(kr[i].z, kr[i].w, kz2f, kz3, ks2f, ks3);
      const int row = 16 * ((i >> 1) & 1) + 4 * (i >> 2) + 2 * (i & 1);
      *(LAS u32x2*)(wr_base + row * KROW) = w;
      __builtin_amdgcn_sched_barrier(0);
      kr[i] = ldb(krs, koff, nxt, i * 1024);
      __builtin_amdgcn_sched_barrier(0);
    }
    f32x4 sc[2];
#pragma unroll
    for (int T = 0; T < 2; ++T) {
      sc[T] = f32x4{0.f, 0.f, 0.f, 0.f};
#pragma unroll
      for (int s = 0; s < 4; ++s) {
        const u32x4 a = *(const LAS u32x4*)(rd_base + T * 16 * KROW + s * 64);
        sc[T] = mfma16(a, qf[s], sc[T]);
      }
    }
    float x[8];
#pragma unroll
    for (int j = 0; j < 8; j += 2) {
      const unsigned u = pk_bf16(sc[j >> 2][j & 3], sc[j >> 2][(j & 3) + 1]);
      const unsigned v = pk_bf16(bf_lo(u) * SCALE, bf_hi(u) * SCALE);
      x[j] = bf_lo(v);
      x[j + 1] = bf_hi(v);
    }
    float bm = fmaxf(fmaxf(fmaxf(x[0], x[1]), fmaxf(x[2], x[3])), fmaxf(fmaxf(x[4], x[5]), fmaxf(x[6], x[7])));
    bm = xg_max(bm);
    const float mn = fmaxf(m, bm);
    const float alpha = __builtin_amdgcn_exp2f((m - mn) * LOG2E);
    m = mn;
    float p[8], ps = 0.f;
#pragma unroll
    for (int j = 0; j < 8; ++j) { p[j] = __builtin_amdgcn_exp2f((x[j] - mn) * LOG2E); ps += p[j]; }
    l = l * alpha + ps;
    u32x4 pf;
#pragma unroll
    for (int j = 0; j < 8; j += 2) {
      const unsigned h = pk_bf16(p[j], p[j + 1]);
      const unsigned lo = pk_bf16(p[j] - bf_lo(h), p[j + 1] - bf_hi(h));
      pf[j >> 1] = (r < 8) ? h : lo;
    }
#pragma unroll
    for (int i = 0; i < 8; ++i) acc[i] *= alpha;
    float vs[8], vz[8];
#pragma unroll
    for (int j = 0; j < 4; ++j) { vs[2 * j] = bf_lo(vs4[j]); vs[2 * j + 1] = bf_hi(vs4[j]); vz[2 * j] = bf_lo(vz4[j]); vz[2 * j + 1] = bf_hi(vz4[j]); }
    __builtin_amdgcn_sched_barrier(0);
    vs4 = __builtin_amdgcn_raw_buffer_load_b128(vsrs, g * 16, (blk + 1) * 64, 0);
    vz4 = __builtin_amdgcn_raw_buffer_load_b128(vzrs, g * 16, (blk + 1) * 64, 0);
    __builtin_amdgcn_sched_barrier(0);
#pragma unroll
    for (int hf = 0; hf < 2; ++hf) {
      u32x4 a[4];
#pragma unroll
      for (int j = 0; j < 8; j += 2) {
        const i32x4 v0 = vr[hf * 8 + j], v1 = vr[hf * 8 + j + 1];
#pragma unroll
        for (int c = 0; c < 4; ++c) a[c][j >> 1] = dq2(v0[c], v1[c], vz[j], vz[j + 1], vs[j], vs[j + 1]);
        __builtin_amdgcn_sched_barrier(0);
        vr[hf * 8 + j] = ldb(vrs, voff, nxt, j * 512 + hf * 256);
        vr[hf * 8 + j + 1] = ldb(vrs, voff, nxt, (j + 1) * 512 + hf * 256);
        __builtin_amdgcn_sched_barrier(0);
      }
#pragma unroll
      for (int c = 0; c < 4; ++c) acc[hf * 4 + c] = mfma16(a[c], pf, acc[hf * 4 + c]);
    }
  }

  l = xg_sum(l);
#pragma unroll
  for (int i = 0; i < 8; ++i)
#pragma unroll
    for (int k = 0; k < 4; ++k) acc[i][k] += ror8(acc[i][k]);
  {
    LAS float* rec = (LAS float*)L;
    const int hf = r >> 3, h = r & 7;
#pragma unroll
    for (int reg = 0; reg < 4; ++reg) {
      f32x4 o;
#pragma unroll
      for (int c = 0; c < 4; ++c) o[c] = hf ? acc[4 + c][reg] : acc[c][reg];
      *(LAS f32x4*)(rec + h * 132 + 64 * hf + 16 * g + 4 * reg) = o;
    }
    if (lane < 8) { rec[8 * 132 + lane] = m; rec[8 * 132 + 8 + lane] = l; }
  }
  __syncthreads();
  {
    const int h = wave, d0 = 2 * lane;
    float mw[NWAVE], M = -INFINITY;
#pragma unroll
    for (int w = 0; w < NWAVE; ++w) { mw[w] = ((LAS float*)((LAS unsigned char*)lds + w * LDS_WAVE))[8 * 132 + h]; M = fmaxf(M, mw[w]); }
    float Ls = 0.f, o0 = 0.f, o1 = 0.f;
#pragma unroll
    for (int w = 0; w < NWAVE; ++w) {
      LAS float* rec = (LAS float*)((LAS unsigned char*)lds + w * LDS_WAVE);
      const float wt = __builtin_amdgcn_exp2f((mw[w] - M) * LOG2E);
      const f32x2 a = *(LAS f32x2*)(rec + h * 132 + d0);
      Ls = fmaf(wt, rec[8 * 132 + 8 + h], Ls);
      o0 = fmaf(wt, a.x, o0);
      o1 = fmaf(wt, a.y, o1);
    }
    const int head = kvh * REP + h, slot = b % WG_PER_HEAD;
    *(f32x2*)(po + ((size_t)head * WG_PER_HEAD + slot) * DH + d0) = f32x2{o0, o1};
    if (lane == 0) *(f32x2*)(pml + ((size_t)head * WG_PER_HEAD + slot) * 2) = f32x2{M, Ls};
  }
}

__global__ void __launch_bounds__(256)
kv_combine_kernel(const float* __restrict__ po, const float* __restrict__ pml, unsigned* __restrict__ out) {
  __shared__ f32x4 red[8][32];
  const int head = blockIdx.x, tid = threadIdx.x, lane = tid & 63;
  const int s8 = tid >> 5, c = tid & 31;
  f32x4 v[8];
#pragma unroll
  for (int i = 0; i < 8; ++i) v[i] = *(const f32x4*)(po + ((size_t)head * WG_PER_HEAD + s8 * 8 + i) * DH + 4 * c);
  const f32x2 ml = *(const f32x2*)(pml + ((size_t)head * WG_PER_HEAD + lane) * 2);
  float M = ml.x;
#pragma unroll
  for (int o = 32; o >= 1; o >>= 1) M = fmaxf(M, __shfl_xor(M, o));
  const float w = __builtin_amdgcn_exp2f((ml.x - M) * 1.4426950408889634f);
  float Ls = w * ml.y;
#pragma unroll
  for (int o = 32; o >= 1; o >>= 1) Ls += __shfl_xor(Ls, o);
  f32x4 a = {0.f, 0.f, 0.f, 0.f};
#pragma unroll
  for (int i = 0; i < 8; ++i) a += v[i] * __shfl(w, s8 * 8 + i);
  red[s8][c] = a;
  __syncthreads();
  if (tid < 32) {
    f32x4 t = red[0][tid];
#pragma unroll
    for (int i = 1; i < 8; ++i) t += red[i][tid];
    const float inv = 1.f / Ls;
    u32x2 o;
    o.x = pk_bf16(t.x * inv, t.y * inv);
    o.y = pk_bf16(t.z * inv, t.w * inv);
    *(u32x2*)(out + head * (DH / 2) + 2 * tid) = o;
  }
}

extern "C" void kernel_launch(void* const* d_in, const int* in_sizes, int n_in, void* d_out, int out_size, void* d_ws,
                              size_t ws_size, hipStream_t stream) {
  const unsigned short* q = (const unsigned short*)d_in[0];
  const int* qk = (const int*)d_in[1];
  const int* qv = (const int*)d_in[2];
  const unsigned short* ksc = (const unsigned short*)d_in[3];
  const unsigned short* kze = (const unsigned short*)d_in[4];
  const unsigned short* vsc = (const unsigned short*)d_in[5];
  const unsigned short* vze = (const unsigned short*)d_in[6];
  float* po = (float*)d_ws;
  float* pml = po + (size_t)HQ * WG_PER_HEAD * DH;
  kv_stream_kernel<<<NWG, NTHR, 0, stream>>>(q, qk, qv, ksc, kze, vsc, vze, po, pml, 1);
  kv_combine_kernel<<<HQ, 256, 0, stream>>>(po, pml, (unsigned*)d_out);
}
```

```cpp
#include <hip/hip_runtime.h>
#include <hip/hip_bf16.h>
#include <stdint.h>

#define GAS __attribute__((address_space(1)))
#define LAS __attribute__((address_space(3)))
typedef unsigned u32x4 __attribute__((ext_vector_type(4)));
typedef unsigned u32x2 __attribute__((ext_vector_type(2)));
typedef float f32x4 __attribute__((ext_vector_type(4)));
typedef float f32x2 __attribute__((ext_vector_type(2)));
typedef __bf16 bf16x2 __attribute__((ext_vector_type(2)));
typedef __bf16 bf16x8 __attribute__((ext_vector_type(8)));

#define SMAP 1

constexpr int HQ = 32, HKV = 4, REP = HQ / HKV, DH = 128, SKV = 65536, GRP = 128, NGK = SKV / GRP;
constexpr int NWAVE = 4, NTHR = NWAVE * 64;
constexpr int NWG = 256;
constexpr int WPH = NWG / HKV;
constexpr int NBLK = NGK / WPH;
constexpr int GRP_BYTES = GRP * DH * 4;
constexpr int STEP_BYTES = WPH * GRP_BYTES;
constexpr int KROW = 272;
constexpr int LDS_KT = 32 * KROW;
constexpr int LDS_VSZ = 2 * NBLK * 32 * 4;
constexpr int LDS_WAVE = LDS_KT + LDS_VSZ;
constexpr int LDS_KSZ = NBLK * 2 * DH * 4;
constexpr int LDS_BYTES = NWAVE * LDS_WAVE + LDS_KSZ + REP * KROW;
constexpr int REC = 132;

__device__ __forceinline__ unsigned pk_bf16(float lo, float hi) {
  f32x2 v = {lo, hi};
  return __builtin_bit_cast(unsigned, __builtin_convertvector(v, bf16x2));
}
__device__ __forceinline__ float rnd_bf16(float x) {
  return __builtin_bit_cast(float, pk_bf16(0.f, x));
}
__device__ __forceinline__ float bf_lo(unsigned u) { return __builtin_bit_cast(float, u << 16); }
__device__ __forceinline__ float bf_hi(unsigned u) { return __builtin_bit_cast(float, u & 0xffff0000u); }
__device__ __forceinline__ f32x4 mfma16(u32x4 a, u32x4 b, f32x4 c) {
  return __builtin_amdgcn_mfma_f32_16x16x32_bf16(__builtin_bit_cast(bf16x8, a), __builtin_bit_cast(bf16x8, b), c, 0, 0, 0);
}
#define CAP 6
__device__ __forceinline__ f32x4 ldb(__amdgpu_buffer_rsrc_t rs, int voff, int n) {
  asm volatile("s_waitcnt vmcnt(%0)" :: "i"(CAP - 1));
  return __builtin_bit_cast(f32x4, __builtin_amdgcn_raw_buffer_load_b128(rs, voff + (n & 3) * 1024, (n >> 2) * 16384, 2));
}
__device__ __forceinline__ float xg_max(float x) {
  auto s = __builtin_amdgcn_permlane16_swap(__float_as_uint(x), __float_as_uint(x), false, false);
  x = fmaxf(__uint_as_float(s[0]), __uint_as_float(s[1]));
  auto t = __builtin_amdgcn_permlane32_swap(__float_as_uint(x), __float_as_uint(x), false, false);
  return fmaxf(__uint_as_float(t[0]), __uint_as_float(t[1]));
}
__device__ __forceinline__ float xg_sum(float x) {
  auto s = __builtin_amdgcn_permlane16_swap(__float_as_uint(x), __float_as_uint(x), false, false);
  x = __uint_as_float(s[0]) + __uint_as_float(s[1]);
  auto t = __builtin_amdgcn_permlane32_swap(__float_as_uint(x), __float_as_uint(x), false, false);
  return __uint_as_float(t[0]) + __uint_as_float(t[1]);
}
__device__ __forceinline__ f32x2 sub_zero(f32x2 xb, f32x2 nz) {
  const f32x2 c = {0x1p126f, 0x1p126f};
  return __builtin_elementwise_fma(xb, c, nz);
}

#define STAMP(base, idx, j)
__global__ void __launch_bounds__(NTHR, 2)
kv_stream_kernel(const unsigned short* __restrict__ q, const int* __restrict__ qk, const int* __restrict__ qv,
                 const unsigned short* __restrict__ ksc, const unsigned short* __restrict__ kze,
                 const unsigned short* __restrict__ vsc, const unsigned short* __restrict__ vze,
                 float* __restrict__ po, float* __restrict__ pml) {
  __shared__ __attribute__((aligned(16))) unsigned char lds[LDS_BYTES];
  const int tid = threadIdx.x, lane = tid & 63;
  const int wave = __builtin_amdgcn_readfirstlane(tid >> 6);
  const int bid = SMAP ? (blockIdx.x & 7) * (NWG / 8) + (blockIdx.x >> 3) : blockIdx.x;
  const int kvh = bid / WPH, slot = bid % WPH;
  const int r = lane & 15, g = lane >> 4, par = lane >> 5, c31 = lane & 31;

  const size_t kvoff = (size_t)kvh * SKV * DH + (size_t)slot * GRP * DH + wave * 8 * DH;
  constexpr int NREC = (NBLK - 1) * STEP_BYTES + GRP_BYTES - 3 * 4096;
  const __amdgpu_buffer_rsrc_t krs = __builtin_amdgcn_make_buffer_rsrc((void*)(qk + kvoff), 0, NREC, 0x00020000);
  const __amdgpu_buffer_rsrc_t vrs = __builtin_amdgcn_make_buffer_rsrc((void*)(qv + kvoff), 0, NREC, 0x00020000);
  int voff = lane * 16;

#define SMALL_POS 6
#define STAGE_POS 12
  const int sblk = 2 * wave + ((lane >> 4) & 1);
  f32x4 rg[16];
#pragma unroll
  for (int n = 0; n < SMALL_POS; ++n) { rg[n] = ldb(krs, voff, n); __builtin_amdgcn_sched_barrier(0); }
  const u32x4 kszr = __builtin_nontemporal_load((const GAS u32x4*)((par ? kze : ksc) + ((size_t)kvh * NGK + sblk * WPH + slot) * DH + 8 * r));
  const u32x4 vszr = __builtin_nontemporal_load((const GAS u32x4*)((par ? vze : vsc) + (size_t)kvh * SKV + (size_t)(((lane >> 2) & 7) * WPH + slot) * GRP + (lane & 3) * 32 + wave * 8));
  const u32x2 qr = __builtin_nontemporal_load((const GAS u32x2*)(q + (size_t)kvh * REP * DH) + tid);
  __builtin_amdgcn_sched_barrier(0);
#pragma unroll
  for (int n = SMALL_POS; n < STAGE_POS; ++n) { rg[n] = ldb(krs, voff, n); __builtin_amdgcn_sched_barrier(0); }

  LAS unsigned char* L = (LAS unsigned char*)lds + wave * LDS_WAVE;
  LAS float* ksz = (LAS float*)((LAS unsigned char*)lds + NWAVE * LDS_WAVE);
  LAS unsigned char* qs = (LAS unsigned char*)lds + NWAVE * LDS_WAVE + LDS_KSZ;
  LAS float* vsz = (LAS float*)(L + LDS_KT);
  {
    const float f = par ? -0x1p-23f : 0x1p23f;
    LAS float* d = ksz + (sblk * 2 + par) * DH + 8 * r;
    *(LAS f32x4*)d = f32x4{bf_lo(kszr.x), bf_hi(kszr.x), bf_lo(kszr.y), bf_hi(kszr.y)} * f;
    *(LAS f32x4*)(d + 4) = f32x4{bf_lo(kszr.z), bf_hi(kszr.z), bf_lo(kszr.w), bf_hi(kszr.w)} * f;
    LAS float* e = vsz + (par * NBLK + ((lane >> 2) & 7)) * 32 + (lane & 3) * 4;
    *(LAS f32x4*)e = f32x4{bf_lo(vszr.x), bf_lo(vszr.y), bf_lo(vszr.z), bf_lo(vszr.w)} * f;
    *(LAS f32x4*)(e + 16) = f32x4{bf_hi(vszr.x), bf_hi(vszr.y), bf_hi(vszr.z), bf_hi(vszr.w)} * f;
    *(LAS u32x2*)(qs + (tid >> 5) * KROW + c31 * 8) = qr;
  }
  __builtin_amdgcn_sched_barrier(0);
#pragma unroll
  for (int n = STAGE_POS; n < 16; ++n) { rg[n] = ldb(krs, voff, n); __builtin_amdgcn_sched_barrier(0); }
  asm volatile("s_waitcnt lgkmcnt(0)" ::: "memory");
  __builtin_amdgcn_s_barrier();
  asm volatile("" ::: "memory");
  u32x4 qf[4];
#pragma unroll
  for (int s = 0; s < 4; ++s) qf[s] = *(const LAS u32x4*)(qs + (r & 7) * KROW + 64 * s + 16 * g);

  LAS unsigned char* wr_base = L + par * 8 * KROW + c31 * 8;
  LAS unsigned char* rd_base = L + r * KROW + g * 16;
  const unsigned mlo = r < 8 ? 0xffffffffu : 0u;

  f32x4 acc[4];
#pragma unroll
  for (int i = 0; i < 4; ++i) acc[i] = f32x4{0.f, 0.f, 0.f, 0.f};
  float m = -INFINITY, l = 0.f;
  constexpr float SCALE = 0.08838834764831845f;
  constexpr float LOG2E = 1.4426950408889634f;

#pragma unroll 1
  for (int blk = 0; blk < NBLK; ++blk) {
    const f32x4 ks = *(const LAS f32x4*)(ksz + blk * 2 * DH + 4 * c31);
    const f32x4 kz = *(const LAS f32x4*)(ksz + blk * 2 * DH + DH + 4 * c31);
#pragma unroll
    for (int n = 0; n < 16; ++n) {
      const f32x2 y0 = sub_zero(rg[n].xy, kz.xy), y1 = sub_zero(rg[n].zw, kz.zw);
      const f32x2 k0 = f32x2{rnd_bf16(y0.x), rnd_bf16(y0.y)} * ks.xy, k1 = f32x2{rnd_bf16(y1.x), rnd_bf16(y1.y)} * ks.zw;
      const u32x2 w = {pk_bf16(k0.x, k0.y), pk_bf16(k1.x, k1.y)};
      const int row = 16 * ((n >> 2) & 1) + 4 * (n >> 3) + (n & 3);
      *(LAS u32x2*)(wr_base + row * KROW) = w;
      __builtin_amdgcn_sched_barrier(0);
      rg[n] = ldb(vrs, voff, n);
      __builtin_amdgcn_sched_barrier(0);
    }
    voff += STEP_BYTES;
    f32x4 sc[2];
#pragma unroll
    for (int T = 0; T < 2; ++T) {
      sc[T] = f32x4{0.f, 0.f, 0.f, 0.f};
#pragma unroll
      for (int s = 0; s < 4; ++s) {
        const u32x4 a = *(const LAS u32x4*)(rd_base + T * 16 * KROW + s * 64);
        sc[T] = mfma16(a, qf[s], sc[T]);
      }
    }
    const LAS float* vsp = vsz + blk * 32 + 8 * g;
    const f32x4 vs0 = *(const LAS f32x4*)vsp, vs1 = *(const LAS f32x4*)(vsp + 4);
    float x[8];
#pragma unroll
    for (int j = 0; j < 8; ++j) x[j] = rnd_bf16(rnd_bf16(sc[j >> 2][j & 3]) * SCALE);
    float bm = fmaxf(fmaxf(fmaxf(x[0], x[1]), fmaxf(x[2], x[3])), fmaxf(fmaxf(x[4], x[5]), fmaxf(x[6], x[7])));
    bm = xg_max(bm);
    const float mn = fmaxf(m, bm);
    const float alpha = __builtin_amdgcn_exp2f((m - mn) * LOG2E);
    m = mn;
    float p[8], ps = 0.f;
#pragma unroll
    for (int j = 0; j < 8; ++j) { p[j] = __builtin_amdgcn_exp2f((x[j] - mn) * LOG2E); ps += p[j]; }
    l = l * alpha + ps;
    u32x4 pf[2];
#pragma unroll
    for (int j = 0; j < 8; j += 2) {
      const float s0 = j < 4 ? vs0[j] : vs1[j - 4], s1 = j < 4 ? vs0[j + 1] : vs1[j - 3];
      const unsigned w = pk_bf16(p[j] * s0, p[j + 1] * s1);
      auto sw = __builtin_amdgcn_permlane16_swap(w & mlo, w & ~mlo, false, false);
      pf[0][j >> 1] = sw[0];
      pf[1][j >> 1] = sw[1];
    }
#pragma unroll
    for (int i = 0; i < 4; ++i) acc[i] *= alpha;
#pragma unroll
    for (int f = 0; f < 2; ++f) {
      const LAS float* vzp = vsz + (NBLK + blk) * 32 + par * 16 + 8 * f;
      const f32x4 vz0 = *(const LAS f32x4*)vzp, vz1 = *(const LAS f32x4*)(vzp + 4);
      u32x4 a[4];
#pragma unroll
      for (int j = 0; j < 8; j += 2) {
        const int n = 8 * f + j;
        const float z0 = j < 4 ? vz0[j] : vz1[j - 4], z1 = j < 4 ? vz0[j + 1] : vz1[j - 3];
        const f32x2 a0 = sub_zero(rg[n].xy, f32x2{z0, z0}), a1 = sub_zero(rg[n].zw, f32x2{z0, z0});
        const f32x2 b0 = sub_zero(rg[n + 1].xy, f32x2{z1, z1}), b1 = sub_zero(rg[n + 1].zw, f32x2{z1, z1});
        a[0][j >> 1] = pk_bf16(a0.x, b0.x);
        a[1][j >> 1] = pk_bf16(a0.y, b0.y);
        a[2][j >> 1] = pk_bf16(a1.x, b1.x);
        a[3][j >> 1] = pk_bf16(a1.y, b1.y);
        __builtin_amdgcn_sched_barrier(0);
        rg[n] = ldb(krs, voff, n);
        __builtin_amdgcn_sched_barrier(0);
        rg[n + 1] = ldb(krs, voff, n + 1);
        __builtin_amdgcn_sched_barrier(0);
      }
#pragma unroll
      for (int c = 0; c < 4; ++c) acc[c] = mfma16(a[c], pf[f], acc[c]);
    }
  }

  l = xg_sum(l);
  {
    LAS float* rec = (LAS float*)L;
#pragma unroll
    for (int reg = 0; reg < 4; ++reg)
      *(LAS f32x4*)(rec + (r & 7) * REC + 64 * (r >> 3) + 16 * g + 4 * reg) = f32x4{acc[0][reg], acc[1][reg], acc[2][reg], acc[3][reg]};
    if (lane < 8) { rec[8 * REC + lane] = m; rec[8 * REC + 8 + lane] = l; }
  }
  __syncthreads();
  {
    const int h = 2 * wave + par;
    float mw[NWAVE], M = -INFINITY;
#pragma unroll
    for (int w = 0; w < NWAVE; ++w) { mw[w] = ((LAS float*)((LAS unsigned char*)lds + w * LDS_WAVE))[8 * REC + h]; M = fmaxf(M, mw[w]); }
    float Ls = 0.f;
    f32x4 o = {0.f, 0.f, 0.f, 0.f};
#pragma unroll
    for (int w = 0; w < NWAVE; ++w) {
      LAS float* rec = (LAS float*)((LAS unsigned char*)lds + w * LDS_WAVE);
      const float wt = __builtin_amdgcn_exp2f((mw[w] - M) * LOG2E);
      Ls = fmaf(wt, rec[8 * REC + 8 + h], Ls);
      o += *(LAS f32x4*)(rec + h * REC + 4 * c31) * wt;
    }
    const int head = kvh * REP + h;
    *(f32x4*)(po + ((size_t)head * WPH + slot) * DH + 4 * c31) = o;
    if (c31 == 0) *(f32x2*)(pml + ((size_t)head * WPH + slot) * 2) = f32x2{M, Ls};
  }
}

__global__ void __launch_bounds__(256)
kv_combine_kernel(const float* __restrict__ po, const float* __restrict__ pml, unsigned* __restrict__ out) {
  __shared__ f32x4 red[8][32];
  const int head = blockIdx.x, tid = threadIdx.x, lane = tid & 63;
  const int s8 = tid >> 5, c = tid & 31;
  f32x4 v[8];
#pragma unroll
  for (int i = 0; i < 8; ++i) v[i] = *(const f32x4*)(po + ((size_t)head * WPH + s8 * 8 + i) * DH + 4 * c);
  const f32x2 ml = *(const f32x2*)(pml + ((size_t)head * WPH + lane) * 2);
  float M = ml.x;
#pragma unroll
  for (int o = 32; o >= 1; o >>= 1) M = fmaxf(M, __shfl_xor(M, o));
  const float w = __builtin_amdgcn_exp2f((ml.x - M) * 1.4426950408889634f);
  float Ls = w * ml.y;
#pragma unroll
  for (int o = 32; o >= 1; o >>= 1) Ls += __shfl_xor(Ls, o);
  f32x4 a = {0.f, 0.f, 0.f, 0.f};
#pragma unroll
  for (int i = 0; i < 8; ++i) a += v[i] * __shfl(w, s8 * 8 + i);
  red[s8][c] = a;
  __syncthreads();
  if (tid < 32) {
    f32x4 t = red[0][tid];
#pragma unroll
    for (int i = 1; i < 8; ++i) t += red[i][tid];
    const float inv = 1.f / Ls;
    u32x2 o;
    o.x = pk_bf16(t.x * inv, t.y * inv);
    o.y = pk_bf16(t.z * inv, t.w * inv);
    *(u32x2*)(out + head * (DH / 2) + 2 * tid) = o;
  }
}

extern "C" void kernel_launch(void* const* d_in, const int* in_sizes, int n_in, void* d_out, int out_size, void* d_ws,
                              size_t ws_size, hipStream_t stream) {
  const unsigned short* q = (const unsigned short*)d_in[0];
  const int* qk = (const int*)d_in[1];
  const int* qv = (const int*)d_in[2];
  const unsigned short* ksc = (const unsigned short*)d_in[3];
  const unsigned short* kze = (const unsigned short*)d_in[4];
  const unsigned short* vsc = (const unsigned short*)d_in[5];
  const unsigned short* vze = (const unsigned short*)d_in[6];
  float* po = (float*)d_ws;
  float* pml = po + (size_t)HQ * WPH * DH;
  kv_stream_kernel<<<NWG, NTHR, 0, stream>>>(q, qk, qv, ksc, kze, vsc, vze, po, pml);
  kv_combine_kernel<<<HQ, 256, 0, stream>>>(po, pml, (unsigned*)d_out);
}
```

```cpp
#include <hip/hip_runtime.h>
#include <hip/hip_bf16.h>
#include <stdint.h>

#define GAS __attribute__((address_space(1)))
#define LAS __attribute__((address_space(3)))
typedef unsigned u32x4 __attribute__((ext_vector_type(4)));
typedef unsigned u32x2 __attribute__((ext_vector_type(2)));
typedef float f32x4 __attribute__((ext_vector_type(4)));
typedef float f32x2 __attribute__((ext_vector_type(2)));
typedef __bf16 bf16x2 __attribute__((ext_vector_type(2)));
typedef __bf16 bf16x8 __attribute__((ext_vector_type(8)));

#define ATOM 0
#define GM_SLEEP 2
#define HELPER_FIRST 0
#define LATE_V 1
#define LATE_SLEEP 0
#define DYN 3568
#define DYN0 0
#define CAPHI 9
#define CUT 0
#define CAPK 6
#define CAPV 6
#define DEFER 3
#define NREP 2
#define SMALL_POS 6
#define STAGE_POS 12

constexpr int HQ = 32, HKV = 4, REP = HQ / HKV, DH = 128, SKV = 65536, GRP = 128, NGK = SKV / GRP;
constexpr int NWAVE = 4, NTHR = (NWAVE + 1) * 64;
constexpr int NWG = 256;
constexpr int WPH = NWG / HKV;
constexpr int NBLK = NGK / WPH;
constexpr int GRP_BYTES = GRP * DH * 4;
constexpr int STEP_BYTES = WPH * GRP_BYTES;
constexpr int KROW = 272;
constexpr int LDS_KT = 32 * KROW;
constexpr int LDS_VSZ = 2 * NBLK * 32 * 4;
constexpr int LDS_X = NBLK * 512;
constexpr int LDS_WAVE = LDS_KT + LDS_VSZ + LDS_X;
constexpr int LDS_KSZ = NBLK * 2 * DH * 4;
constexpr int OFF_KSZ = NWAVE * LDS_WAVE, OFF_Q = OFF_KSZ + LDS_KSZ, OFF_MID = OFF_Q + REP * KROW;
constexpr int LDS_BYTES = OFF_MID + 64 * 4;
constexpr int REC = 132;
constexpr int WS_EPOCH = 0, WS_GM = 4096, WS_ACC = WS_GM + NWG * 64;
constexpr int ACC_HEAD = 144 * 8;
constexpr int ACC_TICKET = NREP * REP * ACC_HEAD;
constexpr int ACC_CHUNK = ((ACC_TICKET + NWAVE * 128 + WPH * 16 - 1) / (WPH * 16)) * 16;
constexpr int ACC_KVH = ACC_CHUNK * WPH;
constexpr int WS_BYTES = WS_ACC + HKV * ACC_KVH;
static_assert(ACC_CHUNK <= 1024 && ACC_KVH % 128 == 0, "");
constexpr int AUX_NT = 2, AUX_SC1 = 16;

__device__ __forceinline__ unsigned pk_bf16(float lo, float hi) {
  f32x2 v = {lo, hi};
  return __builtin_bit_cast(unsigned, __builtin_convertvector(v, bf16x2));
}
__device__ __forceinline__ float rnd_bf16(float x) { return __builtin_bit_cast(float, pk_bf16(0.f, x)); }
__device__ __forceinline__ float bf_lo(unsigned u) { return __builtin_bit_cast(float, u << 16); }
__device__ __forceinline__ float bf_hi(unsigned u) { return __builtin_bit_cast(float, u & 0xffff0000u); }
__device__ __forceinline__ f32x4 mfma16(u32x4 a, u32x4 b, f32x4 c) {
  return __builtin_amdgcn_mfma_f32_16x16x32_bf16(__builtin_bit_cast(bf16x8, a), __builtin_bit_cast(bf16x8, b), c, 0, 0, 0);
}
template <int CAP>
__device__ __forceinline__ f32x4 ldb(__amdgpu_buffer_rsrc_t rs, int voff, int n, int slack) {
  asm volatile("s_waitcnt vmcnt(%1)\n\ts_cmp_lt_i32 %0, 0\n\ts_cbranch_scc1 1f\n\ts_waitcnt vmcnt(%2)\n1:" :: "s"(slack), "i"(CAPHI - 1), "i"(CAP - 1) : "scc");
  return __builtin_bit_cast(f32x4, __builtin_amdgcn_raw_buffer_load_b128(rs, voff + ((n >> 1) & 3) * 1024, (n & 1) * 8192 + (n >> 3) * 32768, AUX_NT));
}
template <int CTRL> __device__ __forceinline__ float dpp(float x) {
  return __builtin_bit_cast(float, __builtin_amdgcn_mov_dpp(__builtin_bit_cast(int, x), CTRL, 0xf, 0xf, true));
}
__device__ __forceinline__ float xg_max(float x) {
  auto s = __builtin_amdgcn_permlane16_swap(__float_as_uint(x), __float_as_uint(x), false, false);
  x = fmaxf(__uint_as_float(s[0]), __uint_as_float(s[1]));
  auto t = __builtin_amdgcn_permlane32_swap(__float_as_uint(x), __float_as_uint(x), false, false);
  return fmaxf(__uint_as_float(t[0]), __uint_as_float(t[1]));
}
__device__ __forceinline__ float xg_sum(float x) {
  auto s = __builtin_amdgcn_permlane16_swap(__float_as_uint(x), __float_as_uint(x), false, false);
  x = __uint_as_float(s[0]) + __uint_as_float(s[1]);
  auto t = __builtin_amdgcn_permlane32_swap(__float_as_uint(x), __float_as_uint(x), false, false);
  return __uint_as_float(t[0]) + __uint_as_float(t[1]);
}
__device__ __forceinline__ float wave_max(float x) {
  x = fmaxf(x, dpp<0xB1>(x)); x = fmaxf(x, dpp<0x4E>(x)); x = fmaxf(x, dpp<0x141>(x)); x = fmaxf(x, dpp<0x128>(x));
  return xg_max(x);
}
__device__ __forceinline__ f32x2 sub_zero(f32x2 xb, f32x2 nz) {
  const f32x2 c = {0x1p126f, 0x1p126f};
  return __builtin_elementwise_fma(xb, c, nz);
}
#define SB() __builtin_amdgcn_sched_barrier(0)
#define RLX_AGENT __ATOMIC_RELAXED, __HIP_MEMORY_SCOPE_AGENT

__global__ void __launch_bounds__(NTHR, 2)
kv_decode_kernel(const unsigned short* __restrict__ q, const int* __restrict__ qk, const int* __restrict__ qv,
                 const unsigned short* __restrict__ ksc, const unsigned short* __restrict__ kze,
                 const unsigned short* __restrict__ vsc, const unsigned short* __restrict__ vze,
                 unsigned char* ws, unsigned* __restrict__ out) {
  __shared__ __attribute__((aligned(16))) unsigned char lds[LDS_BYTES];
  asm volatile("" :: "s"(q), "s"(qk), "s"(qv), "s"(ksc), "s"(kze), "s"(vsc), "s"(vze), "s"(ws), "s"(out));
  const int tid = threadIdx.x, lane = tid & 63;
  const int wave = __builtin_amdgcn_readfirstlane(tid >> 6) - 1;
  const int bid = (blockIdx.x & 7) * (NWG / 8) + (blockIdx.x >> 3);
  const int kvh = bid / WPH, slot = bid % WPH;
  const int r = lane & 15, g = lane >> 4, par = lane >> 5, c31 = lane & 31;

  LAS float* ksz = (LAS float*)((LAS unsigned char*)lds + OFF_KSZ);
  LAS unsigned char* qs = (LAS unsigned char*)lds + OFF_Q;
  volatile LAS float* mid = (volatile LAS float*)((LAS unsigned char*)lds + OFF_MID);
  GAS unsigned* epoch = (GAS unsigned*)(ws + WS_EPOCH + kvh * 128);
  constexpr float LOG2E = 1.4426950408889634f;

  if (HELPER_FIRST ? __builtin_expect(wave < 0, 1) : __builtin_expect(wave < 0, 0)) {
    const __amdgpu_buffer_rsrc_t wrs = __builtin_amdgcn_make_buffer_rsrc((void*)ws, 0, WS_BYTES, 0x00020000);
    u32x4 kszr[4], vszr[4], qr[2];
#pragma unroll
    for (int i = 0; i < 4; ++i)
      kszr[i] = __builtin_nontemporal_load((const GAS u32x4*)((par ? kze : ksc) + ((size_t)kvh * NGK + (2 * i + (g & 1)) * WPH + slot) * DH + 8 * r));
#pragma unroll
    for (int i = 0; i < 2; ++i) qr[i] = __builtin_nontemporal_load((const GAS u32x4*)(q + (size_t)kvh * REP * DH) + i * 64 + lane);
    const float f = par ? -0x1p-23f : 0x1p23f;
    mid[32 + par] = 0.f;
#pragma unroll
    for (int i = 0; i < 4; ++i) {
      LAS float* d = ksz + ((2 * i + (g & 1)) * 2 + par) * DH + 8 * r;
      *(LAS f32x4*)d = f32x4{bf_lo(kszr[i].x), bf_hi(kszr[i].x), bf_lo(kszr[i].y), bf_hi(kszr[i].y)} * f;
      *(LAS f32x4*)(d + 4) = f32x4{bf_lo(kszr[i].z), bf_hi(kszr[i].z), bf_lo(kszr[i].w), bf_hi(kszr[i].w)} * f;
    }
#pragma unroll
    for (int i = 0; i < 2; ++i) *(LAS u32x4*)(qs + (4 * i + g) * KROW + r * 16) = qr[i];
    asm volatile("s_waitcnt lgkmcnt(0)" ::: "memory");
    __builtin_amdgcn_s_barrier();
    asm volatile("" ::: "memory");
    for (int i = 0; i < LATE_SLEEP; ++i) __builtin_amdgcn_s_sleep(64);
#pragma unroll
    for (int i = 0; i < 4; ++i)
      vszr[i] = __builtin_nontemporal_load((const GAS u32x4*)((par ? vze : vsc) + (size_t)kvh * SKV + (size_t)((2 * i + (g & 1)) * WPH + slot) * GRP + 8 * r));
    const unsigned e_in = __hip_atomic_load(epoch, RLX_AGENT);
    LAS float* e0 = (LAS float*)((LAS unsigned char*)lds + ((r & 1) + 2 * ((r >> 2) & 1)) * LDS_WAVE + LDS_KT) + (par * NBLK + (g & 1)) * 32 + ((r >> 1) & 1) + 8 * (r >> 3);
#pragma unroll
    for (int i = 0; i < 4; ++i)
#pragma unroll
      for (int k = 0; k < 4; ++k) { e0[64 * i + 2 * k] = bf_lo(vszr[i][k]) * f; e0[64 * i + 16 + 2 * k] = bf_hi(vszr[i][k]) * f; }
    asm volatile("s_waitcnt lgkmcnt(0)" ::: "memory");
    mid[33] = 1.f;
    const unsigned tag = __builtin_amdgcn_readfirstlane(e_in) + 0x9E3779B9u;
    __builtin_amdgcn_raw_buffer_store_b128(u32x4{0u, 0u, 0u, 0u}, wrs, lane * 16 < ACC_CHUNK ? WS_ACC + bid * ACC_CHUNK + lane * 16 : WS_BYTES, 0, AUX_SC1);
    asm volatile("s_waitcnt vmcnt(0)" ::: "memory");
    while (__float_as_uint(mid[32]) != NWAVE) __builtin_amdgcn_s_sleep(32);
    const float mw = fmaxf(fmaxf(mid[r & 7], mid[8 + (r & 7)]), fmaxf(mid[16 + (r & 7)], mid[24 + (r & 7)]));
    __builtin_amdgcn_raw_buffer_store_b64(u32x2{__float_as_uint(mw), tag}, wrs, lane < 8 ? WS_GM + bid * 64 + lane * 8 : WS_BYTES, 0, AUX_SC1);
    u32x4 gm[4];
#pragma unroll 1
    for (int spins = 0; spins < 200000; ++spins) {
#pragma unroll
      for (int i = 0; i < GM_SLEEP; ++i) __builtin_amdgcn_s_sleep(127);
      bool ok = true;
#pragma unroll
      for (int i = 0; i < 4; ++i) gm[i] = __builtin_amdgcn_raw_buffer_load_b128(wrs, WS_GM + kvh * WPH * 64 + i * 1024 + lane * 16, 0, AUX_SC1);
#pragma unroll
      for (int i = 0; i < 4; ++i) ok = ok && gm[i].y == tag && gm[i].w == tag;
      if (__all(ok)) break;
    }
    float M0 = fmaxf(fmaxf(__uint_as_float(gm[0].x), __uint_as_float(gm[1].x)), fmaxf(__uint_as_float(gm[2].x), __uint_as_float(gm[3].x)));
    float M1 = fmaxf(fmaxf(__uint_as_float(gm[0].z), __uint_as_float(gm[1].z)), fmaxf(__uint_as_float(gm[2].z), __uint_as_float(gm[3].z)));
    M0 = fmaxf(M0, dpp<0x124>(M0)); M0 = fmaxf(M0, dpp<0x128>(M0)); M0 = xg_max(M0);
    M1 = fmaxf(M1, dpp<0x124>(M1)); M1 = fmaxf(M1, dpp<0x128>(M1)); M1 = xg_max(M1);
    mid[40 + 2 * (lane & 3)] = M0;
    mid[41 + 2 * (lane & 3)] = M1;
    if (lane < 32) mid[lane] = __builtin_amdgcn_exp2f((mid[lane] - mid[40 + (lane & 7)]) * LOG2E);
    if (slot == 0 && lane == 0) __hip_atomic_store(epoch, tag, RLX_AGENT);
    asm volatile("s_waitcnt lgkmcnt(0)" ::: "memory");
    __builtin_amdgcn_s_barrier();
    return;
  }

  const int wtok = 8 * (wave & 1) + 32 * (wave >> 1);
  const size_t kvoff = (size_t)kvh * SKV * DH + (size_t)slot * GRP * DH + wtok * DH;
  constexpr int NREC = (NBLK - 1) * STEP_BYTES + 32768 + 8192 + 4096;
  const __amdgpu_buffer_rsrc_t krs = __builtin_amdgcn_make_buffer_rsrc((void*)(qk + kvoff), 0, NREC, 0x00020000);
  const __amdgpu_buffer_rsrc_t vrs = __builtin_amdgcn_make_buffer_rsrc((void*)(qv + kvoff), 0, NREC, 0x00020000);
  int voff = lane * 16;
  unsigned due = (unsigned)__builtin_amdgcn_s_memrealtime() + DYN0;
  f32x4 rg[16];
#pragma unroll
  for (int n = 0; n < 16; ++n) { rg[n] = ldb<CAPK>(krs, voff, n, 0); SB(); }
  LAS unsigned char* L = (LAS unsigned char*)lds + wave * LDS_WAVE;
  LAS float* vsz = (LAS float*)(L + LDS_KT);
  __builtin_amdgcn_s_barrier();
  asm volatile("" ::: "memory");
  u32x4 qf[4];
#pragma unroll
  for (int s = 0; s < 4; ++s) qf[s] = *(const LAS u32x4*)(qs + (r & 7) * KROW + 64 * s + 16 * g);

  LAS unsigned char* wr_base = L + par * 8 * KROW + c31 * 8;
  LAS unsigned char* rd_base = L + r * KROW + g * 16;
  const unsigned mlo = r < 8 ? 0xffffffffu : 0u;
  constexpr float SCALE = 0.08838834764831845f;
  LAS unsigned char* xs = L + LDS_KT + LDS_VSZ + (g * 8 + (r & 7)) * 16;

  float mx = -INFINITY;
#pragma unroll 1
  for (int blk = 0; blk < NBLK; ++blk) {
    const __amdgpu_buffer_rsrc_t nrs = __builtin_amdgcn_make_buffer_rsrc((void*)((blk == NBLK - 1 ? qv : qk) + kvoff), 0, NREC, 0x00020000);
    voff = lane * 16 + ((blk + 1) & (NBLK - 1)) * STEP_BYTES;
    due += DYN / 16;
    const int slack = (int)(due - (unsigned)__builtin_amdgcn_s_memrealtime());
    const f32x4 ks = *(const LAS f32x4*)(ksz + blk * 2 * DH + 4 * c31);
    const f32x4 kz = *(const LAS f32x4*)(ksz + blk * 2 * DH + DH + 4 * c31);
#pragma unroll
    for (int n = 0; n < 16; ++n) {
      const f32x2 y0 = sub_zero(rg[n].xy, kz.xy), y1 = sub_zero(rg[n].zw, kz.zw);
      const f32x2 k0 = f32x2{rnd_bf16(y0.x), rnd_bf16(y0.y)} * ks.xy, k1 = f32x2{rnd_bf16(y1.x), rnd_bf16(y1.y)} * ks.zw;
      const u32x2 w = {pk_bf16(k0.x, k0.y), pk_bf16(k1.x, k1.y)};
      const int row = 16 * ((n >> 2) & 1) + 4 * (n >> 3) + (n & 3);
      *(LAS u32x2*)(wr_base + row * KROW) = w;
      SB();
      if (n < 16 - DEFER) rg[n] = ldb<CAPK>(nrs, voff, n, slack);
      SB();
    }
#define LATE(k) do { if (DEFER >= (k)) { SB(); rg[16 - (k)] = ldb<CAPK>(nrs, voff, 16 - (k), slack); SB(); } } while (0)
    LATE(3);
    f32x4 sc[2];
#pragma unroll
    for (int T = 0; T < 2; ++T) {
      sc[T] = f32x4{0.f, 0.f, 0.f, 0.f};
#pragma unroll
      for (int s = 0; s < 4; ++s) {
        const u32x4 a = *(const LAS u32x4*)(rd_base + T * 16 * KROW + s * 64);
        sc[T] = mfma16(a, qf[s], sc[T]);
      }
    }
    LATE(2);
    u32x4 xp;
#pragma unroll
    for (int j = 0; j < 8; j += 2) {
      const f32x2 t = f32x2{rnd_bf16(sc[j >> 2][j & 3]), rnd_bf16(sc[j >> 2][(j & 3) + 1])} * SCALE;
      mx = fmaxf(mx, fmaxf(t.x, t.y));
      xp[j >> 1] = pk_bf16(t.x, t.y);
    }
    *(LAS u32x4*)(xs + blk * 512) = xp;
    LATE(1);
  }
  const float m = rnd_bf16(xg_max(mx));
  const float nml = -m * LOG2E;
  mid[wave * 8 + (r & 7)] = m;
  if (lane == 0) __hip_atomic_fetch_add((LAS unsigned*)(mid + 32), 1u, __ATOMIC_RELAXED, __HIP_MEMORY_SCOPE_WORKGROUP);

  while (mid[33] != 1.f) __builtin_amdgcn_s_sleep(1);
  asm volatile("" ::: "memory");
  f32x4 acc[4];
#pragma unroll
  for (int i = 0; i < 4; ++i) acc[i] = f32x4{0.f, 0.f, 0.f, 0.f};
  float l = 0.f;
  auto v_block = [&](const int blk, const bool more) __attribute__((always_inline)) {
    voff = lane * 16 + (blk + 1) * STEP_BYTES;
    due += DYN / 16;
    const int slack = more ? (int)(due - (unsigned)__builtin_amdgcn_s_memrealtime()) : 0;
    const u32x4 xp = *(const LAS u32x4*)(xs + blk * 512);
    const LAS float* vsp = vsz + blk * 32 + 8 * g;
    const f32x4 vs0 = *(const LAS f32x4*)vsp, vs1 = *(const LAS f32x4*)(vsp + 4);
    u32x4 pf[2];
#pragma unroll
    for (int f = 0; f < 2; ++f) {
      const LAS float* vzp = vsz + (NBLK + blk) * 32 + par * 16 + 8 * f;
      const f32x4 vz0 = *(const LAS f32x4*)vzp, vz1 = *(const LAS f32x4*)(vzp + 4);
      u32x4 a[4];
#pragma unroll
      for (int j = 0; j < 8; j += 2) {
        const int n = 8 * f + j;
        const float z0 = j < 4 ? vz0[j] : vz1[j - 4], z1 = j < 4 ? vz0[j + 1] : vz1[j - 3];
        const f32x2 a0 = sub_zero(rg[n].xy, f32x2{z0, z0}), a1 = sub_zero(rg[n].zw, f32x2{z0, z0});
        const f32x2 b0 = sub_zero(rg[n + 1].xy, f32x2{z1, z1}), b1 = sub_zero(rg[n + 1].zw, f32x2{z1, z1});
        unsigned e0 = pk_bf16(a0.x, b0.x), e1 = pk_bf16(a0.y, b0.y), e2 = pk_bf16(a1.x, b1.x), e3 = pk_bf16(a1.y, b1.y);
        asm volatile("" : "+v"(e0), "+v"(e1), "+v"(e2), "+v"(e3));
        a[0][j >> 1] = e0; a[1][j >> 1] = e1; a[2][j >> 1] = e2; a[3][j >> 1] = e3;
        SB();
        if (more) rg[n] = ldb<CAPV>(vrs, voff, n, slack);
        SB();
        if (f == 0) {
          const float p0 = __builtin_amdgcn_exp2f(fmaf(bf_lo(xp[j >> 1]), LOG2E, nml));
          const float p1 = __builtin_amdgcn_exp2f(fmaf(bf_hi(xp[j >> 1]), LOG2E, nml));
          l += p0 + p1;
          const float s0 = j < 4 ? vs0[j] : vs1[j - 4], s1 = j < 4 ? vs0[j + 1] : vs1[j - 3];
          const unsigned w = pk_bf16(p0 * s0, p1 * s1);
          auto sw = __builtin_amdgcn_permlane16_swap(w & mlo, w & ~mlo, false, false);
          pf[0][j >> 1] = sw[0];
          pf[1][j >> 1] = sw[1];
          SB();
        }
        if (more) rg[n + 1] = ldb<CAPV>(vrs, voff, n + 1, slack);
        SB();
      }
#pragma unroll
      for (int c = 0; c < 4; ++c) acc[c] = mfma16(a[c], pf[f], acc[c]);
    }
  };
#pragma unroll 1
  for (int blk = 0; blk < NBLK - 1; ++blk) v_block(blk, true);
  v_block(NBLK - 1, false);

  l = xg_sum(l);
  {
    LAS float* rec = (LAS float*)L;
#pragma unroll
    for (int reg = 0; reg < 4; ++reg)
      *(LAS f32x4*)(rec + (r & 7) * REC + 64 * (r >> 3) + 16 * g + 4 * reg) = f32x4{acc[0][reg], acc[1][reg], acc[2][reg], acc[3][reg]};
    rec[(r & 7) * REC + 128] = l;
  }
  asm volatile("s_waitcnt lgkmcnt(0)" ::: "memory");
  __builtin_amdgcn_s_barrier();
  asm volatile("" ::: "memory");
  const int h = 2 * wave + par;
  float wt[NWAVE];
#pragma unroll
  for (int w = 0; w < NWAVE; ++w) wt[w] = mid[w * 8 + h];
  float Ls = 0.f;
  f32x4 o = {0.f, 0.f, 0.f, 0.f};
#pragma unroll
  for (int w = 0; w < NWAVE; ++w) {
    LAS float* rec = (LAS float*)((LAS unsigned char*)lds + w * LDS_WAVE) + h * REC;
    o += *(LAS f32x4*)(rec + 4 * c31) * wt[w];
    Ls = fmaf(wt[w], rec[128], Ls);
  }
  GAS double* ab = (GAS double*)(ws + WS_ACC + kvh * ACC_KVH + h * ACC_HEAD);
  {
    GAS double* a = ab + (slot % NREP) * (REP * ACC_HEAD / 8);
#pragma unroll
    for (int k = 0; k < 4; ++k) __hip_atomic_fetch_add(a + k * 32 + c31, (double)o[k], RLX_AGENT);
    if (c31 == 0) __hip_atomic_fetch_add(a + 128, (double)Ls, RLX_AGENT);
  }
  asm volatile("s_waitcnt vmcnt(0)" ::: "memory");
  unsigned ticket = 0;
  if (lane == 0) ticket = __hip_atomic_fetch_add((GAS unsigned*)(ws + WS_ACC + kvh * ACC_KVH + ACC_TICKET + wave * 128), 1u, RLX_AGENT);
  if (__builtin_amdgcn_readfirstlane(ticket) == WPH - 1) {
    __builtin_amdgcn_fence(__ATOMIC_ACQUIRE, "wavefront");
    double v[NREP][5];
#pragma unroll
    for (int p = 0; p < NREP; ++p) {
#pragma unroll
      for (int k = 0; k < 4; ++k) v[p][k] = __hip_atomic_load(ab + p * (REP * ACC_HEAD / 8) + k * 32 + c31, RLX_AGENT);
      v[p][4] = __hip_atomic_load(ab + p * (REP * ACC_HEAD / 8) + 128, RLX_AGENT);
    }
#pragma unroll
    for (int p = 1; p < NREP; ++p)
#pragma unroll
      for (int k = 0; k < 5; ++k) v[0][k] += v[p][k];
    const float inv = 1.f / (float)v[0][4];
    *(GAS u32x2*)(out + (kvh * REP + h) * (DH / 2) + 2 * c31) =
        u32x2{pk_bf16((float)v[0][0] * inv, (float)v[0][1] * inv), pk_bf16((float)v[0][2] * inv, (float)v[0][3] * inv)};
  }
}

extern "C" void kernel_launch(void* const* d_in, const int* in_sizes, int n_in, void* d_out, int out_size, void* d_ws,
                              size_t ws_size, hipStream_t stream) {
  kv_decode_kernel<<<NWG, NTHR, 0, stream>>>((const unsigned short*)d_in[0], (const int*)d_in[1], (const int*)d_in[2],
                                             (const unsigned short*)d_in[3], (const unsigned short*)d_in[4],
                                             (const unsigned short*)d_in[5], (const unsigned short*)d_in[6],
                                             (unsigned char*)d_ws, (unsigned*)d_out);
}
```

```cpp
#include <hip/hip_runtime.h>
#include <hip/hip_bf16.h>
#include <stdint.h>

#define GAS __attribute__((address_space(1)))
#define LAS __attribute__((address_space(3)))
typedef int i32x4 __attribute__((ext_vector_type(4)));
typedef unsigned u32x4 __attribute__((ext_vector_type(4)));
typedef unsigned u32x2 __attribute__((ext_vector_type(2)));
typedef float f32x4 __attribute__((ext_vector_type(4)));
typedef float f32x2 __attribute__((ext_vector_type(2)));
typedef __bf16 bf16x2 __attribute__((ext_vector_type(2)));
typedef __bf16 bf16x8 __attribute__((ext_vector_type(8)));

constexpr int CAP_LO = 4, CAP_MID = 6, CAP_HI = 8;
constexpr int BLK_TICKS = 218, START_TICKS = 60, BAND = 135;
constexpr float CODE_BOUND = 256.f;

constexpr int HQ = 32, HKV = 4, REP = HQ / HKV, DH = 128, SKV = 65536, GRP = 128, NGK = SKV / GRP;
constexpr int NWAVE = 4, NTHR = (NWAVE + 1) * 64;
constexpr int NWG = 256;
constexpr int WPH = NWG / HKV;
constexpr int NBLK = NGK / WPH;
constexpr int STEP_BYTES = WPH * GRP * DH * 4;
constexpr int KROW = 272;
constexpr int LDS_KT = 32 * KROW;
constexpr int LDS_VSZ = 2 * NBLK * 32 * 4;
constexpr int LDS_X = NBLK * 512;
constexpr int LDS_WAVE = LDS_KT + LDS_VSZ + LDS_X;
constexpr int OFF_KSZ = NWAVE * LDS_WAVE;
constexpr int OFF_Q = OFF_KSZ + NBLK * 2 * DH * 4;
constexpr int OFF_MID = OFF_Q + REP * KROW;
constexpr int MID_CNT = 32, MID_VOK = 33, MID_KOK = 34, MID_DEC = 40, MID_LIM = 48;
constexpr int LDS_BYTES = OFF_MID + 64 * 4;
constexpr int REC = 132;
constexpr int WS_EPOCH = 0, WS_GM = 32768, WS_ACC = 65536, SEG = 256, WS_BYTES = WS_ACC + NWG * SEG;
constexpr int AUX_NT = 2, AUX_SC1 = 16, RSRC3 = 0x00020000;
constexpr float LOG2E = 1.4426950408889634f;

#define SB() __builtin_amdgcn_sched_barrier(0)
#define RLX_AGENT __ATOMIC_RELAXED, __HIP_MEMORY_SCOPE_AGENT
#define WAIT_LDS() asm volatile("s_waitcnt lgkmcnt(0)" ::: "memory")
#define WAIT_VM() asm volatile("s_waitcnt vmcnt(0)" ::: "memory")
#define WG_BARRIER() do { WAIT_LDS(); __builtin_amdgcn_s_barrier(); asm volatile("" ::: "memory"); } while (0)

__device__ __forceinline__ unsigned pk_bf16(float lo, float hi) {
  return __builtin_bit_cast(unsigned, __builtin_convertvector(f32x2{lo, hi}, bf16x2));
}
__device__ __forceinline__ float rnd_bf16(float x) { return __builtin_bit_cast(float, pk_bf16(0.f, x)); }
__device__ __forceinline__ float bf_lo(unsigned u) { return __builtin_bit_cast(float, u << 16); }
__device__ __forceinline__ float bf_hi(unsigned u) { return __builtin_bit_cast(float, u & 0xffff0000u); }
__device__ __forceinline__ f32x2 sub_zero(int x0, int x1, f32x2 nz) { return f32x2{(float)x0, (float)x1} + nz; }
__device__ __forceinline__ f32x4 mfma16(u32x4 a, u32x4 b, f32x4 c) {
  return __builtin_amdgcn_mfma_f32_16x16x32_bf16(__builtin_bit_cast(bf16x8, a), __builtin_bit_cast(bf16x8, b), c, 0, 0, 0);
}
__device__ __forceinline__ i32x4 ld_piece(__amdgpu_buffer_rsrc_t rs, int voff, int n) {
  return __builtin_bit_cast(i32x4, __builtin_amdgcn_raw_buffer_load_b128(rs, voff + ((n >> 1) & 3) * 1024, (n & 1) * 8192 + (n >> 3) * 32768, AUX_NT));
}
__device__ __forceinline__ i32x4 ld_paced(__amdgpu_buffer_rsrc_t rs, int voff, int n, int slack) {
  asm volatile("s_waitcnt vmcnt(%1)\n\ts_cmp_lt_i32 %0, 0\n\ts_cbranch_scc1 1f\n\t"
               "s_waitcnt vmcnt(%2)\n\ts_cmpk_lt_i32 %0, %4\n\ts_cbranch_scc1 1f\n\ts_waitcnt vmcnt(%3)\n1:"
               :: "s"(slack), "i"(CAP_HI - 1), "i"(CAP_MID - 1), "i"(CAP_LO - 1), "i"(BAND) : "scc");
  return ld_piece(rs, voff, n);
}
__device__ __forceinline__ float ror8(float x) {
  return __builtin_bit_cast(float, __builtin_amdgcn_mov_dpp(__builtin_bit_cast(int, x), 0x128, 0xf, 0xf, true));
}
__device__ __forceinline__ float xg_max(float x) {
  auto s = __builtin_amdgcn_permlane16_swap(__float_as_uint(x), __float_as_uint(x), false, false);
  x = fmaxf(__uint_as_float(s[0]), __uint_as_float(s[1]));
  auto t = __builtin_amdgcn_permlane32_swap(__float_as_uint(x), __float_as_uint(x), false, false);
  return fmaxf(__uint_as_float(t[0]), __uint_as_float(t[1]));
}
__device__ __forceinline__ float xg_sum(float x) {
  auto s = __builtin_amdgcn_permlane16_swap(__float_as_uint(x), __float_as_uint(x), false, false);
  x = __uint_as_float(s[0]) + __uint_as_float(s[1]);
  auto t = __builtin_amdgcn_permlane32_swap(__float_as_uint(x), __float_as_uint(x), false, false);
  return __uint_as_float(t[0]) + __uint_as_float(t[1]);
}

__global__ void __launch_bounds__(NTHR, 2)
kv_decode_kernel(const unsigned short* __restrict__ q, const int* __restrict__ qk, const int* __restrict__ qv,
                 const unsigned short* __restrict__ ksc, const unsigned short* __restrict__ kze,
                 const unsigned short* __restrict__ vsc, const unsigned short* __restrict__ vze,
                 unsigned char* ws, unsigned short* out) {
  __shared__ __attribute__((aligned(16))) unsigned char lds_[LDS_BYTES];
  LAS unsigned char* lds = (LAS unsigned char*)lds_;
  asm volatile("" :: "s"(q), "s"(qk), "s"(qv), "s"(ksc), "s"(kze), "s"(vsc), "s"(vze), "s"(ws), "s"(out));
  const int lane = threadIdx.x & 63;
  const int wave = __builtin_amdgcn_readfirstlane(threadIdx.x >> 6) - 1;
  const int bid = (blockIdx.x & 7) * (NWG / 8) + (blockIdx.x >> 3);
  const int kvh = bid / WPH, slot = bid % WPH;
  const int r = lane & 15, g = lane >> 4, par = lane >> 5, c31 = lane & 31;

  const size_t kvoff = (size_t)kvh * SKV * DH + (size_t)slot * GRP * DH + (8 * (wave & 1) + 32 * ((wave >> 1) & 1)) * DH;
  constexpr int NREC = (NBLK - 1) * STEP_BYTES + 32768 + 8192 + 4096;
  const __amdgpu_buffer_rsrc_t krs = __builtin_amdgcn_make_buffer_rsrc((void*)(qk + kvoff), 0, NREC, RSRC3);
  i32x4 rg[16];
  const size_t soff = ((size_t)kvh * NGK + slot) * GRP;
  const unsigned sv = g * (WPH * GRP * 2) + r * 16;
#define LD_SMALL(p, i) __builtin_nontemporal_load((const GAS i32x4*)((const GAS char*)((p) + soff) + (sv + (i) * (4u * WPH * GRP * 2))))
  if (wave >= 0) {
#pragma unroll
    for (int n = 0; n < 6; ++n) { rg[n] = ld_piece(krs, lane * 16, n); SB(); }
  } else {
    rg[0] = LD_SMALL(ksc, 0); rg[1] = LD_SMALL(kze, 0);
    rg[2] = __builtin_nontemporal_load((const GAS i32x4*)(q + (size_t)kvh * REP * DH) + lane);
    rg[3] = __builtin_nontemporal_load((const GAS i32x4*)(q + (size_t)kvh * REP * DH) + 64 + lane);
    rg[4] = LD_SMALL(ksc, 1); rg[5] = LD_SMALL(kze, 1);
  }
  volatile LAS float* mid = (volatile LAS float*)(lds + OFF_MID);
  mid[MID_CNT + g] = 0.f;
  WG_BARRIER();
  int role = wave;
  asm volatile("" : "+s"(role));
  SB();
  LAS float* ksz = (LAS float*)(lds + OFF_KSZ);

  if (role < 0) {
    const __amdgpu_buffer_rsrc_t wrs = __builtin_amdgcn_make_buffer_rsrc((void*)ws, 0, WS_BYTES, RSRC3);
    rg[6] = LD_SMALL(vsc, 0); rg[7] = LD_SMALL(vze, 0); rg[8] = LD_SMALL(vsc, 1); rg[9] = LD_SMALL(vze, 1);
    GAS unsigned* epoch = (GAS unsigned*)(ws + WS_EPOCH + kvh * 128);
    const unsigned e_in = __hip_atomic_load(epoch, RLX_AGENT);
    SB();
#pragma unroll
    for (int i = 0; i < 4; ++i) {
      const u32x4 u = __builtin_bit_cast(u32x4, rg[(i & 1) + 4 * (i >> 1)]) ^ ((i & 1) ? 0x80008000u : 0u);
      LAS float* d = ksz + ((g + 4 * (i >> 1)) * 2 + (i & 1)) * DH + 8 * r;
      *(LAS f32x4*)d = f32x4{bf_lo(u.x), bf_hi(u.x), bf_lo(u.y), bf_hi(u.y)};
      *(LAS f32x4*)(d + 4) = f32x4{bf_lo(u.z), bf_hi(u.z), bf_lo(u.w), bf_hi(u.w)};
    }
#pragma unroll
    for (int i = 0; i < 2; ++i) *(LAS i32x4*)(lds + OFF_Q + (4 * i + g) * KROW + r * 16) = rg[2 + i];
    WAIT_LDS();
    mid[MID_KOK] = 1.f;
    LAS float* e0 = (LAS float*)(lds + ((r & 1) + 2 * ((r >> 2) & 1)) * LDS_WAVE + LDS_KT) + g * 32 + ((r >> 1) & 1) + 8 * (r >> 3);
#pragma unroll
    for (int i = 0; i < 4; ++i) {
      const u32x4 u = __builtin_bit_cast(u32x4, rg[6 + i]) ^ ((i & 1) ? 0x80008000u : 0u);
      LAS float* e = e0 + ((i & 1) * NBLK + 4 * (i >> 1)) * 32;
#pragma unroll
      for (int k = 0; k < 4; ++k) { e[2 * k] = bf_lo(u[k]); e[16 + 2 * k] = bf_hi(u[k]); }
    }
    WAIT_LDS();
    mid[MID_VOK] = 1.f;
    const unsigned tag = __builtin_amdgcn_readfirstlane(e_in) + 0x9E3779B9u;
    __builtin_amdgcn_raw_buffer_store_b128(u32x4{0u, 0u, 0u, 0u}, wrs, lane < SEG / 16 ? WS_ACC + bid * SEG + lane * 16 : WS_BYTES, 0, AUX_SC1);
    WAIT_VM();
    while (__float_as_uint(mid[MID_CNT]) != NWAVE) __builtin_amdgcn_s_sleep(32);
    asm volatile("" ::: "memory");
    const int hh = lane & 7;
    const float mw = fmaxf(fmaxf(mid[hh], mid[8 + hh]), fmaxf(mid[16 + hh], mid[24 + hh]));
    float ls = 0.f, bs = 0.f;
#pragma unroll 2
    for (int i = 0; i < NWAVE * NBLK / 2; ++i) {
      LAS unsigned char* Lw = lds + (i >> 2) * LDS_WAVE + LDS_KT;
      const int blk = 2 * (i & 3) + par;
      const u32x4 xp = *(const LAS u32x4*)(Lw + LDS_VSZ + blk * 512 + c31 * 16);
      const LAS float* sp = (const LAS float*)Lw + blk * 32 + (c31 & 24);
      const f32x4 s0 = *(const LAS f32x4*)sp, s1 = *(const LAS f32x4*)(sp + 4);
      const f32x4 z0 = *(const LAS f32x4*)(sp + NBLK * 32), z1 = *(const LAS f32x4*)(sp + NBLK * 32 + 4);
#pragma unroll
      for (int j = 0; j < 8; ++j) {
        const float p = __builtin_amdgcn_exp2f((((j & 1) ? bf_hi(xp[j >> 1]) : bf_lo(xp[j >> 1])) - mw) * LOG2E);
        ls += p;
        bs = fmaf(p * fabsf(j < 4 ? s0[j] : s1[j - 4]), fabsf(j < 4 ? z0[j] : z1[j - 4]) + CODE_BOUND, bs);
      }
    }
    ls = xg_sum(ls + ror8(ls));
    bs = rnd_bf16(xg_sum(bs + ror8(bs)));
    __builtin_amdgcn_raw_buffer_store_b128(u32x4{pk_bf16(bs, mw), tag, __float_as_uint(ls), tag}, wrs,
                                           lane < 8 ? WS_GM + bid * 128 + lane * 16 : WS_BYTES, 0, AUX_SC1);
    __builtin_amdgcn_s_sleep(100);
    u32x4 gm[8];
#pragma unroll 1
    for (int spins = 0; spins < 1000000; ++spins) {
      bool ok = true;
#pragma unroll
      for (int i = 0; i < 8; ++i)
        gm[i] = __builtin_amdgcn_raw_buffer_load_b128(wrs, WS_GM + kvh * WPH * 128 + (i & 3) * 1024 + lane * 16, (i >> 2) * 4096, AUX_SC1);
#pragma unroll
      for (int i = 0; i < 8; ++i) ok = ok & (gm[i].y == tag) & (gm[i].w == tag);
      if (__all(ok)) break;
      __builtin_amdgcn_s_sleep(32);
      asm volatile("" ::: "memory");
    }
    if (slot == 0 && lane == 0) __hip_atomic_store(epoch, tag, RLX_AGENT);
    float M = bf_hi(gm[0].x);
#pragma unroll
    for (int i = 1; i < 8; ++i) M = fmaxf(M, bf_hi(gm[i].x));
    M = xg_max(fmaxf(M, ror8(M)));
    float Lt = 0.f, Bt = 0.f;
#pragma unroll
    for (int i = 0; i < 8; ++i) {
      const float w = __builtin_amdgcn_exp2f((bf_hi(gm[i].x) - M) * LOG2E);
      Lt = fmaf(w, __uint_as_float(gm[i].z), Lt);
      Bt = fmaf(w, bf_lo(gm[i].x), Bt);
    }
    Lt = xg_sum(Lt + ror8(Lt));
    Bt = xg_sum(Bt + ror8(Bt));
    const float invL = 1.f / Lt;
    const int ex = max(__builtin_amdgcn_frexp_expf(Bt * invL * 1.01f), -60);
    const float up = invL * __builtin_amdgcn_ldexpf(1.f, 27 - ex);
    if (lane < 32) mid[lane] = __builtin_amdgcn_exp2f((mid[lane] - M) * LOG2E) * up;
    if (lane < 8) {
      mid[MID_DEC + lane] = __builtin_amdgcn_ldexpf(1.f, ex - 27);
      mid[MID_LIM + lane] = __builtin_amdgcn_exp2f((mw - M) * LOG2E) * bs * up;
    }
    WG_BARRIER();
    return;
  }

  __builtin_amdgcn_s_setprio(2);
  unsigned due = (unsigned)__builtin_amdgcn_s_memrealtime() + START_TICKS;
#pragma unroll
  for (int n = 6; n < 16; ++n) {
    asm volatile("s_waitcnt vmcnt(5)");
    rg[n] = ld_piece(krs, lane * 16, n);
    SB();
  }
  LAS unsigned char* L = lds + wave * LDS_WAVE;
  while (mid[MID_KOK] != 1.f) __builtin_amdgcn_s_sleep(1);
  asm volatile("" ::: "memory");
  u32x4 qf[4];
#pragma unroll
  for (int s = 0; s < 4; ++s) qf[s] = *(const LAS u32x4*)(lds + OFF_Q + (r & 7) * KROW + 64 * s + 16 * g);
  LAS unsigned char* xs = L + LDS_KT + LDS_VSZ + (g * 8 + (r & 7)) * 16;

  float mx = -INFINITY;
#pragma unroll 1
  for (int blk = 0; blk < NBLK; ++blk) {
    const __amdgpu_buffer_rsrc_t nrs = __builtin_amdgcn_make_buffer_rsrc((void*)((blk == NBLK - 1 ? qv : qk) + kvoff), 0, NREC, RSRC3);
    const int voff = lane * 16 + ((blk + 1) & (NBLK - 1)) * STEP_BYTES;
    due += BLK_TICKS;
    const int slack = (int)(due - (unsigned)__builtin_amdgcn_s_memrealtime());
    const f32x4 ks = *(const LAS f32x4*)(ksz + blk * 2 * DH + 4 * c31);
    const f32x4 kz = *(const LAS f32x4*)(ksz + blk * 2 * DH + DH + 4 * c31);
#pragma unroll
    for (int n = 0; n < 16; ++n) {
      const f32x2 y0 = sub_zero(rg[n].x, rg[n].y, kz.xy), y1 = sub_zero(rg[n].z, rg[n].w, kz.zw);
      const f32x2 k0 = f32x2{rnd_bf16(y0.x), rnd_bf16(y0.y)} * ks.xy, k1 = f32x2{rnd_bf16(y1.x), rnd_bf16(y1.y)} * ks.zw;
      *(LAS u32x2*)(L + (8 * par + 16 * ((n >> 2) & 1) + 4 * (n >> 3) + (n & 3)) * KROW + c31 * 8) = u32x2{pk_bf16(k0.x, k0.y), pk_bf16(k1.x, k1.y)};
      SB();
      if (n < 13) rg[n] = ld_paced(nrs, voff, n, slack);
      SB();
    }
    rg[13] = ld_paced(nrs, voff, 13, slack);
    SB();
    f32x4 sc[2];
#pragma unroll
    for (int T = 0; T < 2; ++T) {
      sc[T] = f32x4{0.f, 0.f, 0.f, 0.f};
#pragma unroll
      for (int s = 0; s < 4; ++s) sc[T] = mfma16(*(const LAS u32x4*)(L + (16 * T + r) * KROW + 64 * s + 16 * g), qf[s], sc[T]);
    }
    SB();
    rg[14] = ld_paced(nrs, voff, 14, slack);
    SB();
    u32x4 xp;
#pragma unroll
    for (int j = 0; j < 8; j += 2) {
      const f32x2 t = f32x2{rnd_bf16(sc[j >> 2][j & 3]), rnd_bf16(sc[j >> 2][(j & 3) + 1])} * 0.08838834764831845f;
      mx = fmaxf(mx, fmaxf(t.x, t.y));
      xp[j >> 1] = pk_bf16(t.x, t.y);
    }
    *(LAS u32x4*)(xs + blk * 512) = xp;
    SB();
    rg[15] = ld_paced(nrs, voff, 15, slack);
    SB();
  }
  const float m = rnd_bf16(xg_max(mx));
  asm volatile("" ::: "memory");
  mid[wave * 8 + (r & 7)] = m;
  if (lane == 0) __hip_atomic_fetch_add((LAS unsigned*)(mid + MID_CNT), 1u, __ATOMIC_RELAXED, __HIP_MEMORY_SCOPE_WORKGROUP);

  while (mid[MID_VOK] != 1.f) __builtin_amdgcn_s_sleep(1);
  asm volatile("" ::: "memory");
  const __amdgpu_buffer_rsrc_t vrs = __builtin_amdgcn_make_buffer_rsrc((void*)(qv + kvoff), 0, NREC, RSRC3);
  const LAS float* vsz = (const LAS float*)(L + LDS_KT);
  const unsigned mlo = r < 8 ? 0xffffffffu : 0u;
  f32x4 acc[4];
#pragma unroll
  for (int c = 0; c < 4; ++c) acc[c] = f32x4{0.f, 0.f, 0.f, 0.f};
  auto v_block = [&](const int blk, const bool more) __attribute__((always_inline)) {
    const int voff = lane * 16 + (blk + 1) * STEP_BYTES;
    due += BLK_TICKS;
    const int slack = more ? (int)(due - (unsigned)__builtin_amdgcn_s_memrealtime()) : 0;
    const u32x4 xp = *(const LAS u32x4*)(xs + blk * 512);
    const f32x4 vs0 = *(const LAS f32x4*)(vsz + blk * 32 + 8 * g), vs1 = *(const LAS f32x4*)(vsz + blk * 32 + 8 * g + 4);
    u32x4 pf[2];
#pragma unroll
    for (int f = 0; f < 2; ++f) {
      const LAS float* vzp = vsz + (NBLK + blk) * 32 + par * 16 + 8 * f;
      const f32x4 vz0 = *(const LAS f32x4*)vzp, vz1 = *(const LAS f32x4*)(vzp + 4);
      u32x4 a[4];
#pragma unroll
      for (int j = 0; j < 8; j += 2) {
        const int n = 8 * f + j;
        const float z0 = j < 4 ? vz0[j] : vz1[j - 4], z1 = j < 4 ? vz0[j + 1] : vz1[j - 3];
        const f32x2 a0 = sub_zero(rg[n].x, rg[n].y, f32x2{z0, z0}), a1 = sub_zero(rg[n].z, rg[n].w, f32x2{z0, z0});
        const f32x2 b0 = sub_zero(rg[n + 1].x, rg[n + 1].y, f32x2{z1, z1}), b1 = sub_zero(rg[n + 1].z, rg[n + 1].w, f32x2{z1, z1});
        unsigned e0 = pk_bf16(a0.x, b0.x), e1 = pk_bf16(a0.y, b0.y), e2 = pk_bf16(a1.x, b1.x), e3 = pk_bf16(a1.y, b1.y);
        asm volatile("" : "+v"(e0), "+v"(e1), "+v"(e2), "+v"(e3));
        a[0][j >> 1] = e0; a[1][j >> 1] = e1; a[2][j >> 1] = e2; a[3][j >> 1] = e3;
        SB();
        if (more) rg[n] = ld_paced(vrs, voff, n, slack);
        SB();
        if (f == 0) {
          const float p0 = __builtin_amdgcn_exp2f((bf_lo(xp[j >> 1]) - m) * LOG2E);
          const float p1 = __builtin_amdgcn_exp2f((bf_hi(xp[j >> 1]) - m) * LOG2E);
          const unsigned w = pk_bf16(p0 * (j < 4 ? vs0[j] : vs1[j - 4]), p1 * (j < 4 ? vs0[j + 1] : vs1[j - 3]));
          auto sw = __builtin_amdgcn_permlane16_swap(w & mlo, w & ~mlo, false, false);
          pf[0][j >> 1] = sw[0];
          pf[1][j >> 1] = sw[1];
          SB();
        }
        if (more) rg[n + 1] = ld_paced(vrs, voff, n + 1, slack);
        SB();
      }
#pragma unroll
      for (int c = 0; c < 4; ++c) acc[c] = mfma16(a[c], pf[f], acc[c]);
    }
  };
#pragma unroll 1
  for (int blk = 0; blk < NBLK - 1; ++blk) v_block(blk, true);
  const int h = 2 * wave + par;
  LAS unsigned char* rec_wr = L + ((r & 7) * REC + 64 * (r >> 3) + 16 * g) * 4;
  LAS unsigned char* rec_rd = lds + (h * REC + 4 * c31) * 4;
  LAS float* fac_rd = (LAS float*)mid + h;
  GAS unsigned char* seg = (GAS unsigned char*)ws + WS_ACC + ((kvh * REP + h) * 8 + (c31 >> 3)) * SEG + (c31 & 7) * 8;
  int o_off = ((kvh * REP + h) * DH + 16 * (c31 >> 2) + (c31 & 3)) * 2;
  asm volatile("" : "+v"(rec_wr), "+v"(rec_rd), "+v"(fac_rd), "+v"(seg), "+v"(o_off));
  const __amdgpu_buffer_rsrc_t ors = __builtin_amdgcn_make_buffer_rsrc((void*)out, 0, HQ * DH * 2, RSRC3);
  v_block(NBLK - 1, false);

#pragma unroll
  for (int c = 0; c < 4; ++c) *(LAS f32x4*)(rec_wr + 16 * c) = acc[c];
  WG_BARRIER();
  f32x4 part[NWAVE];
  float fac[NWAVE];
#pragma unroll
  for (int w = 0; w < NWAVE; ++w) { part[w] = *(LAS f32x4*)(rec_rd + w * LDS_WAVE); fac[w] = fac_rd[w * 8]; }
  const float dec = fac_rd[MID_DEC], lim = fac_rd[MID_LIM];
  f32x4 o = part[0] * fac[0];
#pragma unroll
  for (int w = 1; w < NWAVE; ++w) o += part[w] * fac[w];
  unsigned long long zero64 = 0;
  asm volatile("" : "+s"(zero64));
  unsigned long long inside = ~0ull;
#pragma unroll
  for (int k = 0; k < 4; ++k) inside &= __builtin_amdgcn_ballot_w64(fabsf(o[k]) <= lim);
  if (__builtin_expect(inside != ~0ull, 0)) {
#pragma unroll
    for (int k = 0; k < 4; ++k)
      __hip_atomic_fetch_add((GAS double*)(seg + 128 + (c31 & 7) * 8 + (k >> 1) * 4 * SEG) + (k & 1), (double)o[k], RLX_AGENT);
    WAIT_VM();
#pragma unroll
    for (int k = 0; k < 2; ++k) __hip_atomic_fetch_or((GAS unsigned long long*)(seg + k * 4 * SEG), 1ull, RLX_AGENT);
    WAIT_VM();
    o = f32x4{0.f, 0.f, 0.f, 0.f};
  }
  unsigned long long sum[2];
#pragma unroll
  for (int k = 0; k < 2; ++k) {
    const unsigned long long add = ((unsigned long long)(long long)(int)rintf(o[2 * k]) << 29) +
                                   (unsigned long long)(long long)((int)rintf(o[2 * k + 1]) * 2) + (1ull << 58);
    sum[k] = add + __hip_atomic_fetch_add((GAS unsigned long long*)(seg + k * 4 * SEG), add, RLX_AGENT);
  }
  float val[4];
  bool last[2];
#pragma unroll
  for (int k = 0; k < 2; ++k) {
    val[2 * k] = (float)(int)(unsigned)((sum[k] + (1ull << 28)) >> 29);
    val[2 * k + 1] = (float)((int)((unsigned)sum[k] << 3) >> 4);
    last[k] = (sum[k] + (1ull << 57)) >> 58 == 0;
  }
  if (__builtin_expect(__builtin_amdgcn_ballot_w64(((sum[0] | sum[1]) & 1) != 0) != 0, 0)) {
#pragma unroll
    for (int k = 0; k < 4; ++k)
      val[k] = (float)((double)val[k] + __builtin_bit_cast(double, __hip_atomic_fetch_or(
                   (GAS unsigned long long*)(seg + 128 + (c31 & 7) * 8 + (k >> 1) * 4 * SEG) + (k & 1), zero64, RLX_AGENT)));
  }
#pragma unroll
  for (int k = 0; k < 2; ++k) {
    const unsigned pk = pk_bf16(val[2 * k] * dec, val[2 * k + 1] * dec);
    const int off = last[k] ? o_off + 16 * k : HQ * DH * 2;
    __builtin_amdgcn_raw_buffer_store_b16((unsigned short)pk, ors, off, 0, 0);
    __builtin_amdgcn_raw_buffer_store_b16((unsigned short)(pk >> 16), ors, off + 8, 0, 0);
  }
}

extern "C" void kernel_launch(void* const* d_in, const int* in_sizes, int n_in, void* d_out, int out_size, void* d_ws,
                              size_t ws_size, hipStream_t stream) {
  kv_decode_kernel<<<NWG, NTHR, 0, stream>>>((const unsigned short*)d_in[0], (const int*)d_in[1], (const int*)d_in[2],
                                             (const unsigned short*)d_in[3], (const unsigned short*)d_in[4],
                                             (const unsigned short*)d_in[5], (const unsigned short*)d_in[6],
                                             (unsigned char*)d_ws, (unsigned short*)d_out);
}
```
